```python
import torch

def _softmax(x, axis=-1):
    return torch.softmax(x, dim=axis)
def _gelu(x):
    return torch.nn.functional.gelu(x, approximate="tanh")
def _silu(x):
    return torch.nn.functional.silu(x)
def _sigmoid(x):
    return torch.sigmoid(x)
def _layernorm(x, axis=-1, eps=1e-5):
    m = x.mean(dim=axis, keepdim=True)
    v = x.var(dim=axis, keepdim=True, unbiased=False)
    return (x - m) / torch.sqrt(v + eps)
def _scatter_add(tgt, idx, src, axis=0):
    return tgt.clone().index_add_(axis, idx, src)
def _segment_sum(src, seg, n):
    out = torch.zeros((n,) + src.shape[1:], dtype=src.dtype)
    return out.index_add_(0, seg, src)
def _pad(x, pw):
    flat = tuple(v for pair in reversed(pw) for v in pair)
    return torch.nn.functional.pad(x, flat)

kernel_name = 'parallel_sum__mha_block_cached__swiglu_ffn__decode'

def setup_inputs():

    _w = (lambda a: (lambda t: t.to(torch.bfloat16) if t.is_floating_point() else t)(torch.as_tensor(a)))
    def _A_setup():
        B, Sq, D = 4, 1, 512
        H, Sk, Dh = 32, 8192, 128
        return {
            "x": _w((lambda *s: torch.randn(*s, dtype=torch.bfloat16) * 0.05)(B, Sq, D)),
            "k_cache": _w((lambda *s: torch.randn(*s, dtype=torch.bfloat16) * 3.2)(B, H, Sk, Dh)),
            "v_cache": _w((lambda *s: torch.randn(*s, dtype=torch.bfloat16) * 1.6)(B, H, Sk, Dh)),
            "wq": _w((lambda *s: torch.randn(*s, dtype=torch.bfloat16) * 3.2)(D, H * Dh) * (D ** -0.5)),
            "wo": _w((lambda *s: torch.randn(*s, dtype=torch.bfloat16) * 1.6)(H * Dh, D) * ((H * Dh) ** -0.5)),
        }
    _A_in = _A_setup()

    def _B_setup():
        B, S, D, F = 4, 1, 512, 1792
        return {
            "x": _w((lambda *s: torch.randn(*s, dtype=torch.bfloat16) * 0.05)(B, S, D)),
            "w_gate": _w((lambda *s: torch.randn(*s, dtype=torch.bfloat16) * 0.05)(D, F)),
            "w_up": _w((lambda *s: torch.randn(*s, dtype=torch.bfloat16) * 0.05)(D, F)),
            "w_down": _w((lambda *s: torch.randn(*s, dtype=torch.bfloat16) * 0.05)(F, D)),
        }
    _B_in = _B_setup()

    return {
        "A_x": _A_in["x"],
        "A_k_cache": _A_in["k_cache"],
        "A_v_cache": _A_in["v_cache"],
        "A_wq": _A_in["wq"],
        "A_wo": _A_in["wo"],
        "B_x": _B_in["x"],
        "B_w_gate": _B_in["w_gate"],
        "B_w_up": _B_in["w_up"],
        "B_w_down": _B_in["w_down"],
    }

def reference(A_x, A_k_cache, A_v_cache, A_wq, A_wo, B_x, B_w_gate, B_w_up, B_w_down):
    def _A_ref(x, k_cache, v_cache, wq, wo):
        B, Sq, D = x.shape
        H, Dh = 32, 128
        q = (lambda x, *d: x.permute(*d))((x @ wq).reshape(B, Sq, H, Dh), 0, 2, 1, 3)
        scores = torch.einsum("bhqd,bhkd->bhqk", q, k_cache) * (Dh ** -0.5)
        p = _softmax(scores, axis=-1)
        o = torch.einsum("bhqk,bhkd->bhqd", p, v_cache)
        return (lambda x, *d: x.permute(*d))(o, 0, 2, 1, 3).reshape(B, Sq, H * Dh) @ wo

    def _B_ref(x, w_gate, w_up, w_down):
        g = _silu(x @ w_gate)
        u = x @ w_up
        return (g * u) @ w_down

    x = A_x
    xn = _layernorm(x, axis=-1)
    return x + _A_ref(xn, A_k_cache, A_v_cache, A_wq, A_wo) + _B_ref(xn, B_w_gate, B_w_up, B_w_down)
```

```cpp
#include <hip/hip_runtime.h>
#include <hip/hip_bf16.h>
#include <stdint.h>

typedef unsigned u32x4 __attribute__((ext_vector_type(4)));
typedef unsigned u32x2 __attribute__((ext_vector_type(2)));
typedef float f32x2 __attribute__((ext_vector_type(2)));
typedef float f32x4 __attribute__((ext_vector_type(4)));
typedef __attribute__((ext_vector_type(2))) __bf16 bf16x2_t;
#define GAS __attribute__((address_space(1)))
typedef GAS unsigned gu32;
typedef GAS unsigned long long gu64;
#define RLX_AGENT __ATOMIC_RELAXED, __HIP_MEMORY_SCOPE_AGENT

#define KD 3
#define DBG_TS 0

constexpr int DM = 512, NH = 32, SK = 8192, DH = 128, FF = 1792;
constexpr int NWG = 256, NTHR = 512;
constexpr int D = KD;
#define KP 6
constexpr int P = KP;
constexpr int WN = 16 + P;
constexpr int EX0 = 1, EX1 = 6;
static_assert(EX1 + 1 + D < 16 && WN + D <= 32, "schedule");
constexpr int ex_wait(int J) { int n = D - 1; for (int k = J - D; k < J; ++k) n += (k == EX0 || k == EX1) ? 1 : 0; return n; }
constexpr int ka_issue(int n) { return n < D ? WN + n : (n >= WN ? n + D : -1); }
constexpr int ka_pos(int i) {
  if (i < 16) return 2 + 2 * i;
  if (i < WN) return 18 + i;
  int pos = 18 + WN;
  for (int n = 0; n < 32; ++n) { if (ka_issue(n) == i) return pos; pos += (ka_issue(n) >= 0 ? 1 : 0) + (n == 0 ? 1 : 0); }
  return -1;
}
constexpr int ka_wait(int n) {
  int cnt = 18 + WN;
  for (int k = 0; k < n; ++k) cnt += (ka_issue(k) >= 0 ? 1 : 0) + (k == 0 ? 1 : 0);
  const int v = cnt - 1 - ka_pos(n);
  return v > 63 ? 63 : v;
}
constexpr int R = D < 4 ? 4 : 8;
constexpr int ITB = 262144;
constexpr unsigned SPIN_CAP = 1u << 20;

constexpr size_t WS_EPOCH = 0;
constexpr size_t WS_LSE = 4096;
constexpr size_t WS_G = 65536;
constexpr size_t WS_TS = WS_G + (size_t)4 * 64 * 128 * 64;

constexpr int L_QP = 0;
constexpr int L_RED = 1024;
constexpr int L_GU = 2048;
constexpr int L_CNT = 2560;
constexpr int L_FLOATS = 21504;

template <int OFF> __device__ __forceinline__ void ld16_nt(u32x4& d, unsigned voff, const void* sb) {
  asm volatile("global_load_dwordx4 %0, %1, %2 offset:%3 nt" : "=v"(d) : "v"(voff), "s"(sb), "i"(OFF) : "memory");
}
template <int OFF> __device__ __forceinline__ void ld16(u32x4& d, unsigned voff, const void* sb) {
  asm volatile("global_load_dwordx4 %0, %1, %2 offset:%3" : "=v"(d) : "v"(voff), "s"(sb), "i"(OFF) : "memory");
}
template <int N, int OFF> __device__ __forceinline__ void wait_ld_nt(u32x4& cur, u32x4& nxt, unsigned voff, const void* sb) {
  asm volatile("s_waitcnt vmcnt(%4)\n\tglobal_load_dwordx4 %1, %2, %3 offset:%5 nt" : "+v"(cur), "+v"(nxt) : "v"(voff), "s"(sb), "i"(N), "i"(OFF) : "memory");
}
#define SBAR() __builtin_amdgcn_sched_barrier(0)
template <int N> __device__ __forceinline__ void wait_vm(u32x4& r) { asm volatile("s_waitcnt vmcnt(%1)" : "+v"(r) : "i"(N) : "memory"); }
template <int N> __device__ __forceinline__ void wait_vm2(u32x4& r, unsigned& a) { asm volatile("s_waitcnt vmcnt(%2)" : "+v"(r), "+v"(a) : "i"(N) : "memory"); }
__device__ __forceinline__ void wg_barrier() { asm volatile("s_waitcnt lgkmcnt(0)\n\ts_barrier" ::: "memory"); }

template <int CTRL> __device__ __forceinline__ float dpp(float x) {
  return __builtin_bit_cast(float, __builtin_amdgcn_mov_dpp(__builtin_bit_cast(int, x), CTRL, 0xf, 0xf, true));
}
constexpr int XOR1 = 0xB1, XOR2 = 0x4E, HMIR = 0x141, RMIR = 0x140, ROR4 = 0x124, ROR8 = 0x128, BCAST = 0x150;
__device__ __forceinline__ float lo16(unsigned u) { return __builtin_bit_cast(float, u << 16); }
__device__ __forceinline__ float hi16(unsigned u) { return __builtin_bit_cast(float, u & 0xffff0000u); }
__device__ __forceinline__ float dot2(unsigned k, unsigned q, float acc) {
  return __builtin_amdgcn_fdot2_f32_bf16(__builtin_bit_cast(bf16x2_t, k), __builtin_bit_cast(bf16x2_t, q), acc, false);
}
__device__ __forceinline__ unsigned pack_bf16(float lo, float hi) {
  bf16x2_t v = {(__bf16)lo, (__bf16)hi};
  return __builtin_bit_cast(unsigned, v);
}
__device__ __forceinline__ float swap32_add(float a, float b) {
  auto s = __builtin_amdgcn_permlane32_swap(__float_as_uint(a), __float_as_uint(b), false, false);
  return __uint_as_float(s[0]) + __uint_as_float(s[1]);
}
__device__ __forceinline__ float swap16_add(float a, float b) {
  auto s = __builtin_amdgcn_permlane16_swap(__float_as_uint(a), __float_as_uint(b), false, false);
  return __uint_as_float(s[0]) + __uint_as_float(s[1]);
}
__device__ __forceinline__ float swap32_max(float a) {
  auto s = __builtin_amdgcn_permlane32_swap(__float_as_uint(a), __float_as_uint(a), false, false);
  return fmaxf(__uint_as_float(s[0]), __uint_as_float(s[1]));
}
__device__ __forceinline__ float swap16_max(float a) {
  auto s = __builtin_amdgcn_permlane16_swap(__float_as_uint(a), __float_as_uint(a), false, false);
  return fmaxf(__uint_as_float(s[0]), __uint_as_float(s[1]));
}
__device__ __forceinline__ float row_sum(float x) { x += dpp<XOR1>(x); x += dpp<XOR2>(x); x += dpp<HMIR>(x); x += dpp<RMIR>(x); return x; }
__device__ __forceinline__ float row_max(float x) {
  x = fmaxf(x, dpp<XOR1>(x)); x = fmaxf(x, dpp<XOR2>(x)); x = fmaxf(x, dpp<HMIR>(x)); x = fmaxf(x, dpp<RMIR>(x)); return x;
}
__device__ __forceinline__ float wave_sum(float x) { x = row_sum(x); x = swap16_add(x, x); return swap32_add(x, x); }
__device__ __forceinline__ float wave_max(float x) { x = row_max(x); x = swap16_max(x); return swap32_max(x); }
struct Acc8 { f32x2 a[4]; };
__device__ __forceinline__ void acc_zero(Acc8& acc) {
#pragma unroll
  for (int k = 0; k < 4; ++k) acc.a[k] = f32x2{0.f, 0.f};
}
__device__ __forceinline__ void fma8(Acc8& acc, float s, const u32x4& w) {
  const f32x2 s2 = {s, s};
#pragma unroll
  for (int k = 0; k < 4; ++k) acc.a[k] = __builtin_elementwise_fma(s2, f32x2{lo16(w[k]), hi16(w[k])}, acc.a[k]);
  asm volatile("" : "+v"(acc.a[0]), "+v"(acc.a[1]), "+v"(acc.a[2]), "+v"(acc.a[3]));
}
__device__ __forceinline__ void xrow_reduce8(const Acc8& x, float& u0, float& u1) {
  const float t0 = swap32_add(x.a[0][0], x.a[2][0]), t1 = swap32_add(x.a[0][1], x.a[2][1]), t2 = swap32_add(x.a[1][0], x.a[3][0]), t3 = swap32_add(x.a[1][1], x.a[3][1]);
  u0 = swap16_add(t0, t2); u1 = swap16_add(t1, t3);
}
template <int T> struct IC { static constexpr int v = T; };
template <int I, int N, class F> __device__ __forceinline__ void static_for(F&& f) { if constexpr (I < N) { f(IC<I>{}); static_for<I + 1, N>(f); } }

struct Args {
  const char* x; const char* k; const char* v; const char* wq; const char* wo; const char* wg; const char* wu; const char* wd;
  char* out; char* ws;
};

__global__ void __launch_bounds__(NTHR, 2) fused_decode_kernel(Args a) {
  __shared__ __attribute__((aligned(16))) float lds[L_FLOATS];
  const int tid = threadIdx.x, lane = tid & 63, w = __builtin_amdgcn_readfirstlane(tid >> 6);
  const int g = blockIdx.x, xcd = g & 7;
  const int s = (g >> 3) & 1, hh = (g >> 4) & 3, b = g >> 6, h = hh * 8 + xcd;
  const int r = lane >> 4, c = lane & 15;
  const int pidx = h * 2 + s;
  const int cg = hh * 2 + s;
  const int f0 = xcd * 224 + (cg < 7 ? cg : 6) * 32;
  const int cb = w & 3, rh = w >> 2;
#define STAMP(i) do { } while (0)
#define STAMPR(i) do { } while (0)
#define STAMP_FLUSH(n) do { } while (0)
  STAMP(0);

  u32x4 xc; unsigned xo, ep;
  {
    const char* xb = a.x + b * (DM * 2);
    ld16<0>(xc, lane * 16, xb);
    asm volatile("global_load_ushort %0, %1, %2" : "=v"(xo) : "v"((unsigned)(lane * 2)), "s"(xb + w * 128) : "memory");
  }
  const size_t kvoff = (size_t)(b * NH + h) * SK * (DH * 2) + s * 32768 + w * 4096;
  const char* kb = a.k + kvoff;
  const char* vb = a.v + kvoff;
  unsigned vo4[4];
#pragma unroll
  for (int i = 0; i < 4; ++i) vo4[i] = lane * 16 + i * 65536;
  u32x4 win[32];
  u32x4 wr[16];
  u32x4 rs[R];
#pragma unroll
  for (int i = 0; i < R; ++i) rs[i] = u32x4{0u, 0u, 0u, 0u};
  const char* wqb = a.wq + (size_t)(w * 64) * (NH * DH * 2) + h * (DH * 2);
  const unsigned voq = (unsigned)(r * 16) * (NH * DH * 2) + c * 16;
  auto issue_k = [&](auto i) {
    constexpr int I = i.v;
    ld16_nt<(I & 3) * 1024>(win[I], vo4[(I & 15) >> 2], kb + (I >> 4) * ITB);
  };
  auto issue_wq = [&](auto t) { ld16_nt<0>(wr[t.v], voq, wqb + t.v * (NH * DH * 2)); };
  static_for<0, P>([&](auto i) { issue_k(i); issue_wq(i); });
  if (tid == 0) lds[L_CNT] = 0.f;

  wait_vm2<2 * P>(xc, xo);
  STAMPR(1);
  float xn;
  {
    float xv[8];
#pragma unroll
    for (int k = 0; k < 4; ++k) { xv[2 * k] = lo16(xc[k]); xv[2 * k + 1] = hi16(xc[k]); }
    float sm = ((xv[0] + xv[1]) + (xv[2] + xv[3])) + ((xv[4] + xv[5]) + (xv[6] + xv[7]));
    const float mean = wave_sum(sm) * (1.0f / DM);
    float sq = 0.f;
#pragma unroll
    for (int k = 0; k < 8; ++k) { const float d = xv[k] - mean; sq = fmaf(d, d, sq); }
    const float var = wave_sum(sq) * (1.0f / DM);
    const float rstd = 1.0f / sqrtf(var + 1e-5f);
    xn = (lo16(xo) - mean) * rstd;
  }
  unsigned qh[4], ql[4];
  {
    Acc8 acc; acc_zero(acc);
    SBAR();
    static_for<0, 16>([&](auto t) {
      constexpr int T = t.v;
      constexpr int cnt = 2 + 2 * (P + T < 16 ? P + T : 16) + (P + T > 16 ? P + T - 16 : 0);
      wait_vm<cnt - 4 - 2 * T>(wr[T]);
      issue_k(IC<P + T>{});
      if constexpr (P + T < 16) issue_wq(IC<P + T>{});
      fma8(acc, dpp<BCAST + T>(xn), wr[T]);
      SBAR();
    });
    float u0, u1;
    STAMPR(2);
    xrow_reduce8(acc, u0, u1);
    *reinterpret_cast<f32x2*>(&lds[L_QP + w * 128 + 8 * c + 2 * r]) = f32x2{u0, u1};
    wg_barrier();
    float q0 = 0.f, q1 = 0.f;
#pragma unroll
    for (int i = 0; i < 8; ++i) { const f32x2 t = *reinterpret_cast<const f32x2*>(&lds[L_QP + i * 128 + 8 * c + 2 * r]); q0 += t[0]; q1 += t[1]; }
    constexpr float QS = 0.08838834764831845f * 1.4426950408889634f;
    q0 *= QS; q1 *= QS;
    const unsigned ph = pack_bf16(q0, q1);
    const unsigned pl = pack_bf16(q0 - lo16(ph), q1 - hi16(ph));
    {
      auto s1 = __builtin_amdgcn_permlane16_swap(ph, ph, false, false);
      auto e = __builtin_amdgcn_permlane32_swap(s1[0], s1[0], false, false);
      auto o = __builtin_amdgcn_permlane32_swap(s1[1], s1[1], false, false);
      qh[0] = e[0]; qh[2] = e[1]; qh[1] = o[0]; qh[3] = o[1];
    }
    {
      auto s1 = __builtin_amdgcn_permlane16_swap(pl, pl, false, false);
      auto e = __builtin_amdgcn_permlane32_swap(s1[0], s1[0], false, false);
      auto o = __builtin_amdgcn_permlane32_swap(s1[1], s1[1], false, false);
      ql[0] = e[0]; ql[2] = e[1]; ql[1] = o[0]; ql[3] = o[1];
    }
  }
  STAMP(3);

  float sc[8];
#pragma unroll
  for (int i = 0; i < 8; ++i) sc[i] = 0.f;
  const bool b0 = (c & 1) != 0, b1 = (c & 2) != 0;
  const int cq = c >> 2;
  auto reduce4 = [&](const float (&p)[4]) -> float {
    const float k0 = b0 ? p[1] : p[0], s0 = b0 ? p[0] : p[1];
    const float k1 = b0 ? p[3] : p[2], s1 = b0 ? p[2] : p[3];
    const float a0 = k0 + dpp<XOR1>(s0), a1 = k1 + dpp<XOR1>(s1);
    const float k2 = b1 ? a1 : a0, s2 = b1 ? a0 : a1;
    float t = k2 + dpp<XOR2>(s2);
    t += dpp<ROR4>(t); t += dpp<ROR8>(t);
    return t;
  };
  auto kdot = [&](const u32x4& kv) -> float {
    float d0 = dot2(kv[0], qh[0], 0.f), d1 = dot2(kv[0], ql[0], 0.f);
#pragma unroll
    for (int k = 1; k < 4; ++k) { d0 = dot2(kv[k], qh[k], d0); d1 = dot2(kv[k], ql[k], d1); }
    return d0 + d1;
  };
  {
    float part[4], snew = 0.f;
    static_for<0, 32>([&](auto j) {
      constexpr int N = j.v, I = ka_issue(N);
      if constexpr (I >= 32) wait_ld_nt<ka_wait(N), (I & 3) * 1024>(win[N], rs[I % R], vo4[(I & 15) >> 2], kb + 2 * ITB);
      else { wait_vm<ka_wait(N)>(win[N]); if constexpr (I >= 0) issue_k(IC<(I >= 0 ? I : 0)>{}); }
      if constexpr (N == 0) asm volatile("global_load_dword %0, %1, %2 sc1" : "=v"(ep) : "v"(0u), "s"(a.ws + WS_EPOCH + b * 256) : "memory");
      part[N & 3] = kdot(win[N]);
      if constexpr ((N & 3) == 3) { const float t = reduce4(part); snew = (cq == ((N & 15) >> 2)) ? t : snew; }
      if constexpr (N == 15) sc[6] = snew;
      SBAR();
    });
    sc[7] = snew;
  }
  {
    Acc8 ag, au; acc_zero(ag); acc_zero(au);
    float xq[4];
    static_for<0, 4>([&](auto t) { xq[t.v] = dpp<t.v * 0x55>(xn); });
    const unsigned vog = (unsigned)((lane >> 2) * 4) * (FF * 2) + (lane & 3) * 16;
    const size_t sog = (size_t)(w * 64) * (FF * 2) + f0 * 2;
    for (int it = 2; it < 6; ++it) {
      const char* pc = kb + (size_t)it * ITB;
      const char* pn = pc + ITB;
      float part[4], snew = 0.f;
      u32x4 gt, ut;
      static_for<0, 16>([&](auto j) {
        constexpr int J = j.v;
        wait_ld_nt<ex_wait(J), ((J + D) & 3) * 1024>(rs[J % R], rs[(J + D) % R], vo4[((J + D) & 15) >> 2], (J + D >= 16) ? pn : pc);
        if constexpr (J == EX0) ld16<0>(gt, vog, a.wg + sog + (it - 2) * (FF * 2));
        if constexpr (J == EX1) ld16<0>(ut, vog, a.wu + sog + (it - 2) * (FF * 2));
        if constexpr (J == EX0 + 1 + D) { asm volatile("" : "+v"(gt)); fma8(ag, xq[0], gt); }
        if constexpr (J == EX1 + 1 + D) { asm volatile("" : "+v"(ut)); fma8(au, xq[0], ut); }
        part[J & 3] = kdot(rs[J % R]);
        if constexpr ((J & 3) == 3) { const float t = reduce4(part); snew = (cq == (J >> 2)) ? t : snew; }
        SBAR();
      });
#pragma unroll
      for (int i = 0; i < 7; ++i) sc[i] = sc[i + 1];
      sc[7] = snew;
      xq[0] = xq[1]; xq[1] = xq[2]; xq[2] = xq[3];
    }
    float t8[8], t4[4];
#pragma unroll
    for (int k = 0; k < 8; ++k) t8[k] = swap32_add(ag.a[k >> 1][k & 1], au.a[k >> 1][k & 1]);
#pragma unroll
    for (int k = 0; k < 4; ++k) { float t = swap16_add(t8[k], t8[k + 4]); t += dpp<ROR4>(t); t += dpp<ROR8>(t); t4[k] = t; }
    if ((lane & 12) == 0) {
      const int fb = (r >> 1) * 32 + 8 * (lane & 3) + 4 * (r & 1);
#pragma unroll
      for (int k = 0; k < 4; ++k) lds[L_GU + (fb + k) * 8 + w] = t4[k];
    }
    asm volatile("s_waitcnt lgkmcnt(0)" ::: "memory");
    if (lane == 0) __hip_atomic_fetch_add(reinterpret_cast<unsigned*>(lds) + L_CNT, 1u, __ATOMIC_RELAXED, __HIP_MEMORY_SCOPE_WORKGROUP);
  }
  u32x4 wdr[4];
#pragma unroll
  for (int i = 0; i < 4; ++i) wdr[i] = u32x4{0u, 0u, 0u, 0u};
  {
    const char* wdb = a.wd + (size_t)(f0 + rh * 16) * (DM * 2) + cb * 256;
    const unsigned vod = (unsigned)(r * 4) * (DM * 2) + c * 16;
    for (int it = 6; it < 8; ++it) {
      const char* pc = kb + (size_t)it * ITB;
      const char* pn = it < 7 ? pc + ITB : vb;
      float part[4], snew = 0.f;
      wdr[0] = wdr[2]; wdr[1] = wdr[3];
      static_for<0, 16>([&](auto j) {
        constexpr int J = j.v;
        wait_ld_nt<ex_wait(J), ((J + D) & 3) * 1024>(rs[J % R], rs[(J + D) % R], vo4[((J + D) & 15) >> 2], (J + D >= 16) ? pn : pc);
        if constexpr (J == EX0) ld16<0>(wdr[2], vod, wdb + (it - 6) * 2048);
        if constexpr (J == EX1) ld16<1024>(wdr[3], vod, wdb + (it - 6) * 2048);
        if constexpr (J == EX1 + 1 + D) asm volatile("" : "+v"(wdr[2]), "+v"(wdr[3]));
        part[J & 3] = kdot(rs[J % R]);
        if constexpr ((J & 3) == 3) { const float t = reduce4(part); snew = (cq == (J >> 2)) ? t : snew; }
        SBAR();
      });
#pragma unroll
      for (int i = 0; i < 7; ++i) sc[i] = sc[i + 1];
      sc[7] = snew;
    }
  }
  STAMP(4);
  asm volatile("" : "+v"(ep));
  const unsigned tag = __builtin_amdgcn_readfirstlane(ep) + 1u;
  float mw;
  {
    float m = fmaxf(fmaxf(fmaxf(sc[0], sc[1]), fmaxf(sc[2], sc[3])), fmaxf(fmaxf(sc[4], sc[5]), fmaxf(sc[6], sc[7])));
    mw = wave_max(m);
    float l = 0.f;
#pragma unroll
    for (int i = 0; i < 8; ++i) { sc[i] = __builtin_amdgcn_exp2f(sc[i] - mw); l += sc[i]; }
    l = wave_sum(l);
    const float lse = mw + __builtin_amdgcn_logf(l);
    if (lane == 0)
      __hip_atomic_store((gu64*)(a.ws + WS_LSE) + (b * NH + h) * 16 + s * 8 + w, ((unsigned long long)tag << 32) | __float_as_uint(lse), RLX_AGENT);
  }
  Acc8 ay; acc_zero(ay);
  {
    for (unsigned spins = 0; spins < SPIN_CAP; ++spins) {
      const unsigned cnt = __hip_atomic_load(reinterpret_cast<unsigned*>(lds) + L_CNT, __ATOMIC_RELAXED, __HIP_MEMORY_SCOPE_WORKGROUP);
      if (__builtin_amdgcn_readfirstlane(cnt) >= 8u) break;
      __builtin_amdgcn_s_sleep(1);
    }
    asm volatile("" ::: "memory");
    const int f = rh * 16 + r * 4 + (c & 3);
    const f32x4 g0 = *reinterpret_cast<const f32x4*>(&lds[L_GU + f * 8]), g1 = *reinterpret_cast<const f32x4*>(&lds[L_GU + f * 8 + 4]);
    const f32x4 u0 = *reinterpret_cast<const f32x4*>(&lds[L_GU + (32 + f) * 8]), u1 = *reinterpret_cast<const f32x4*>(&lds[L_GU + (32 + f) * 8 + 4]);
    const float gs = ((g0[0] + g0[1]) + (g0[2] + g0[3])) + ((g1[0] + g1[1]) + (g1[2] + g1[3]));
    const float us = ((u0[0] + u0[1]) + (u0[2] + u0[3])) + ((u1[0] + u1[1]) + (u1[2] + u1[3]));
    const float hv = gs / (1.0f + __expf(-gs)) * us;
    if (cg < 7) {
      static_for<0, 4>([&](auto t) { fma8(ay, dpp<BCAST + t.v>(hv), wdr[t.v]); });
    }
  }
  Acc8 acc; acc_zero(acc);
  float coef = 0.f;
  u32x2 lg;
  const gu64* lsep = (const gu64*)(a.ws + WS_LSE) + (b * NH + h) * 16 + c;
  const char* wob = a.wo + (size_t)(h * DH + rh * 64) * (DM * 2) + cb * 256;
  const unsigned vow = (unsigned)(r * 16) * (DM * 2) + c * 16;
#pragma unroll
  for (int i = 0; i < 16; ++i) wr[i] = u32x4{0u, 0u, 0u, 0u};
  for (int it = 0; it < 7; ++it) {
    const char* pc = vb + (size_t)it * ITB;
    const char* pn = pc + ITB;
    const float pcur = sc[0];
    static_for<0, 16>([&](auto j) {
      constexpr int J = j.v;
      wait_ld_nt<ex_wait(J), ((J + D) & 3) * 1024>(rs[J % R], rs[(J + D) % R], vo4[((J + D) & 15) >> 2], (J + D >= 16) ? pn : pc);
      if constexpr (J == EX0) ld16<0>(wr[14], vow, wob + it * 2048);
      if constexpr (J == EX1) ld16<1024>(wr[15], vow, wob + it * 2048);
      if constexpr (J == EX1 + 1 + D) asm volatile("" : "+v"(wr[14]), "+v"(wr[15]));
      fma8(acc, dpp<BCAST + J>(pcur), rs[J % R]);
      SBAR();
    });
#pragma unroll
    for (int i = 0; i < 7; ++i) sc[i] = sc[i + 1];
#pragma unroll
    for (int i = 0; i < 14; ++i) wr[i] = wr[i + 2];
  }
  {
    asm volatile("global_load_dwordx2 %0, %1, off sc1" : "=v"(lg) : "v"(lsep) : "memory");
    const char* pc = vb + (size_t)7 * ITB;
    const float pcur = sc[0];
    static_for<0, 16>([&](auto j) {
      constexpr int J = j.v;
      if constexpr (J + D < 16) wait_ld_nt<ex_wait(J), ((J + D) & 3) * 1024>(rs[J % R], rs[(J + D) % R], vo4[((J + D) & 15) >> 2], pc);
      else wait_vm<15 - J>(rs[J % R]);
      if constexpr (J == EX0) ld16<0>(wr[14], vow, wob + 7 * 2048);
      if constexpr (J == EX1) ld16<1024>(wr[15], vow, wob + 7 * 2048);
      if constexpr (J == EX1 + 1 + D) asm volatile("" : "+v"(wr[14]), "+v"(wr[15]));
      if constexpr (J == 8) {
        asm volatile("" : "+v"(lg));
        unsigned lv = lg[0], lt = lg[1];
        for (unsigned spins = 0; spins < SPIN_CAP; ++spins) {
          if (__all(lt == tag)) break;
          __builtin_amdgcn_s_sleep(8);
          const unsigned long long v = __hip_atomic_load(lsep, RLX_AGENT);
          lv = (unsigned)v; lt = (unsigned)(v >> 32);
        }
        const float le = __uint_as_float(lv);
        const float M = row_max(le);
        const float L = row_sum(__builtin_amdgcn_exp2f(le - M));
        coef = __builtin_amdgcn_exp2f(mw - M) / L;
      }
      fma8(acc, dpp<BCAST + J>(pcur), rs[J % R]);
      SBAR();
    });
  }
  STAMP(5);
  {
    float u0, u1;
    xrow_reduce8(acc, u0, u1);
    *reinterpret_cast<f32x2*>(&lds[L_RED + w * 128 + 8 * c + 2 * r]) = f32x2{u0 * coef, u1 * coef};
    wg_barrier();
    float ov = 0.f;
#pragma unroll
    for (int i = 0; i < 8; ++i) ov += lds[L_RED + i * 128 + rh * 64 + lane];
    static_for<0, 16>([&](auto t) { fma8(ay, dpp<BCAST + t.v>(ov), wr[t.v]); });
    float y0, y1;
    xrow_reduce8(ay, y0, y1);
    gu64* gp = (gu64*)(a.ws + WS_G) + ((size_t)((b * 64 + cb * 16 + c) * 128 + pidx * 2 + rh)) * 8 + r;
    __hip_atomic_store(gp, ((unsigned long long)tag << 32) | __float_as_uint(y0), RLX_AGENT);
    __hip_atomic_store(gp + 4, ((unsigned long long)tag << 32) | __float_as_uint(y1), RLX_AGENT);
  }
  STAMP(6);
  STAMP_FLUSH(7);
  if (w != 0) return;
  {
    const gu64* gp = (const gu64*)(a.ws + WS_G) + (size_t)(b * 64 + pidx) * 1024 + lane;
    unsigned val[16];
    bool ok = false;
    for (unsigned spins = 0; spins < SPIN_CAP; ++spins) {
      if (!ok) {
        bool o = true;
#pragma unroll
        for (int i = 0; i < 16; ++i) { const unsigned long long v = __hip_atomic_load(gp + i * 64, RLX_AGENT); val[i] = (unsigned)v; o &= (unsigned)(v >> 32) == tag; }
        ok = o;
      }
      if (__all(ok)) break;
      __builtin_amdgcn_s_sleep(4);
    }
    float t = 0.f;
#pragma unroll
    for (int i = 0; i < 16; ++i) t += __uint_as_float(val[i]);
    t += dpp<ROR8>(t);
    t = swap16_add(t, t);
    t = swap32_add(t, t);
    if (lane < 8) {
      const int col = 8 * pidx + 2 * (lane & 3) + (lane >> 2);
      const float xv = lo16(reinterpret_cast<const unsigned short*>(a.x)[b * DM + col]);
      reinterpret_cast<__bf16*>(a.out)[b * DM + col] = (__bf16)(xv + t);
    }
    if (pidx == 0 && lane == 0) __hip_atomic_store((gu32*)(a.ws + WS_EPOCH + b * 256), tag, RLX_AGENT);
  }
  STAMP(7);
  STAMP_FLUSH(8);
}

extern "C" void kernel_launch(void* const* d_in, const int* in_sizes, int n_in, void* d_out, int out_size, void* d_ws, size_t ws_size,
                              hipStream_t stream) {
  Args a;
  a.x = (const char*)d_in[0]; a.k = (const char*)d_in[1]; a.v = (const char*)d_in[2]; a.wq = (const char*)d_in[3]; a.wo = (const char*)d_in[4];
  a.wg = (const char*)d_in[6]; a.wu = (const char*)d_in[7]; a.wd = (const char*)d_in[8];
  a.out = (char*)d_out; a.ws = (char*)d_ws;
  hipLaunchKernelGGL(fused_decode_kernel, dim3(NWG), dim3(NTHR), 0, stream, a);
}
```

```cpp
#include <hip/hip_runtime.h>
#include <hip/hip_bf16.h>
#include <stdint.h>

typedef unsigned u32x4 __attribute__((ext_vector_type(4)));
typedef unsigned u32x2 __attribute__((ext_vector_type(2)));
typedef float f32x2 __attribute__((ext_vector_type(2)));
typedef float f32x4 __attribute__((ext_vector_type(4)));
typedef __attribute__((ext_vector_type(2))) __bf16 bf16x2_t;
#define GAS __attribute__((address_space(1)))
typedef GAS unsigned gu32;
typedef GAS unsigned long long gu64;
#define RLX_AGENT __ATOMIC_RELAXED, __HIP_MEMORY_SCOPE_AGENT
#define RLX_WG __ATOMIC_RELAXED, __HIP_MEMORY_SCOPE_WORKGROUP

#define KD 8
#define KA 50
#define KHD 8
#define DBG_TS 0

constexpr int DM = 512, NH = 32, SK = 8192, DH = 128, FF = 1792;
constexpr int NWG = 256, NTHR = 512, NSW = 8;
#define TAIL 2
#define GSLEEP 4
#define MSLEEP 48
#define MSH 8
#define KDM 6
#define KDL 4
#define TOFF 80
#define TAUK 240
#define TAUV 222
#define TMARG 30
constexpr int D = KD, R = 16, A = KA, HD = KHD;
constexpr int DMID = KDM, DLO = KDL;
constexpr int ITB = 131072;
constexpr int NIT = 16;
constexpr int NUN = ((2 * (A - D) + 15) / 16) * 16;
constexpr unsigned SPIN_CAP = 1u << 20;
static_assert(D < R && NUN <= 16 * (NIT - 1), "schedule");

constexpr size_t WS_EPOCH = 0;
constexpr size_t WS_LSE = 4096;
constexpr size_t WS_MEAN = 32768;
constexpr size_t WS_PROG = 65536;
constexpr size_t WS_G = 262144;
constexpr size_t WS_TS = WS_G + (size_t)4 * 64 * 128 * 64;

constexpr int L_QP = 0;
constexpr int L_RED = 512;
constexpr int L_GU = 1024;
constexpr int L_YF = 1280;
constexpr int L_CNT = 1792;
constexpr int L_VT = 1800;
constexpr int L_WO = 2048;
constexpr int L_FLOATS = 21504;

template <int OFF> __device__ __forceinline__ void ld16_nt(u32x4& d, unsigned voff, const void* sb) {
  asm volatile("global_load_dwordx4 %0, %1, %2 offset:%3 nt" : "=v"(d) : "v"(voff), "s"(sb), "i"(OFF) : "memory");
}
template <int N, int OFF> __device__ __forceinline__ void pace_ld_nt(u32x4& nxt, unsigned voff, const void* sb) {
  asm volatile("s_waitcnt vmcnt(%3)\n\tglobal_load_dwordx4 %0, %1, %2 offset:%4 nt" : "=v"(nxt) : "v"(voff), "s"(sb), "i"(N), "i"(OFF) : "memory");
}
template <int N, int OFF> __device__ __forceinline__ void wait_ld_nt(u32x4& cur, u32x4& nxt, unsigned voff, const void* sb) {
  asm volatile("s_waitcnt vmcnt(%4)\n\tglobal_load_dwordx4 %1, %2, %3 offset:%5 nt" : "+v"(cur), "+v"(nxt) : "v"(voff), "s"(sb), "i"(N), "i"(OFF) : "memory");
}
template <int OFF> __device__ __forceinline__ void wait_ld_thr(u32x4& cur, u32x4& nxt, unsigned voff, const void* sb, int lvl) {
  asm volatile("s_cmp_lt_i32 %4, 1\n\ts_cbranch_scc1 .Lthr%=\n\ts_waitcnt vmcnt(%6)\n\ts_cmp_lt_i32 %4, 2\n\ts_cbranch_scc1 .Lthr%=\n\ts_waitcnt vmcnt(%7)\n.Lthr%=:\n\t"
               "s_waitcnt vmcnt(%5)\n\tglobal_load_dwordx4 %1, %2, %3 offset:%8 nt"
               : "+v"(cur), "+v"(nxt) : "v"(voff), "s"(sb), "s"(lvl), "i"(D - 1), "i"(DMID - 1), "i"(DLO - 1), "i"(OFF) : "memory", "scc");
}
#define SBAR() __builtin_amdgcn_sched_barrier(0)
template <int N> __device__ __forceinline__ void wait_vm(u32x4& r) { asm volatile("s_waitcnt vmcnt(%1)" : "+v"(r) : "i"(N) : "memory"); }
__device__ __forceinline__ void landed(u32x4& r) { asm volatile("" : "+v"(r)); }
__device__ __forceinline__ void wg_barrier() { asm volatile("s_waitcnt lgkmcnt(0)\n\ts_barrier" ::: "memory"); }

template <int CTRL> __device__ __forceinline__ float dpp(float x) {
  return __builtin_bit_cast(float, __builtin_amdgcn_mov_dpp(__builtin_bit_cast(int, x), CTRL, 0xf, 0xf, true));
}
constexpr int XOR1 = 0xB1, XOR2 = 0x4E, HMIR = 0x141, RMIR = 0x140, ROR4 = 0x124, ROR8 = 0x128, BCAST = 0x150;
__device__ __forceinline__ float lo16(unsigned u) { return __builtin_bit_cast(float, u << 16); }
__device__ __forceinline__ float hi16(unsigned u) { return __builtin_bit_cast(float, u & 0xffff0000u); }
__device__ __forceinline__ float dot2(unsigned k, unsigned q, float acc) {
  return __builtin_amdgcn_fdot2_f32_bf16(__builtin_bit_cast(bf16x2_t, k), __builtin_bit_cast(bf16x2_t, q), acc, false);
}
__device__ __forceinline__ unsigned pack_bf16(float lo, float hi) {
  bf16x2_t v = {(__bf16)lo, (__bf16)hi};
  return __builtin_bit_cast(unsigned, v);
}
__device__ __forceinline__ float swap32_add(float a, float b) {
  auto s = __builtin_amdgcn_permlane32_swap(__float_as_uint(a), __float_as_uint(b), false, false);
  return __uint_as_float(s[0]) + __uint_as_float(s[1]);
}
__device__ __forceinline__ float swap16_add(float a, float b) {
  auto s = __builtin_amdgcn_permlane16_swap(__float_as_uint(a), __float_as_uint(b), false, false);
  return __uint_as_float(s[0]) + __uint_as_float(s[1]);
}
__device__ __forceinline__ float swap32_max(float a) {
  auto s = __builtin_amdgcn_permlane32_swap(__float_as_uint(a), __float_as_uint(a), false, false);
  return fmaxf(__uint_as_float(s[0]), __uint_as_float(s[1]));
}
__device__ __forceinline__ float swap16_max(float a) {
  auto s = __builtin_amdgcn_permlane16_swap(__float_as_uint(a), __float_as_uint(a), false, false);
  return fmaxf(__uint_as_float(s[0]), __uint_as_float(s[1]));
}
__device__ __forceinline__ float row_sum(float x) { x += dpp<XOR1>(x); x += dpp<XOR2>(x); x += dpp<HMIR>(x); x += dpp<RMIR>(x); return x; }
__device__ __forceinline__ float row_max(float x) {
  x = fmaxf(x, dpp<XOR1>(x)); x = fmaxf(x, dpp<XOR2>(x)); x = fmaxf(x, dpp<HMIR>(x)); x = fmaxf(x, dpp<RMIR>(x)); return x;
}
__device__ __forceinline__ float wave_sum(float x) { x = row_sum(x); x = swap16_add(x, x); return swap32_add(x, x); }
__device__ __forceinline__ float wave_max(float x) { x = row_max(x); x = swap16_max(x); return swap32_max(x); }
struct Acc8 { f32x2 a[4]; };
__device__ __forceinline__ void acc_zero(Acc8& acc) {
#pragma unroll
  for (int k = 0; k < 4; ++k) acc.a[k] = f32x2{0.f, 0.f};
}
__device__ __forceinline__ void pin(Acc8& acc) { asm volatile("" : "+v"(acc.a[0]), "+v"(acc.a[1]), "+v"(acc.a[2]), "+v"(acc.a[3])); }
__device__ __forceinline__ void fma8(Acc8& acc, float s, const u32x4& w) {
  const f32x2 s2 = {s, s};
#pragma unroll
  for (int k = 0; k < 4; ++k) acc.a[k] = __builtin_elementwise_fma(s2, f32x2{lo16(w[k]), hi16(w[k])}, acc.a[k]);
  pin(acc);
}
__device__ __forceinline__ void unpack8(Acc8& o, const u32x4& w) {
#pragma unroll
  for (int k = 0; k < 4; ++k) o.a[k] = f32x2{lo16(w[k]), hi16(w[k])};
  pin(o);
}
__device__ __forceinline__ void xrow_reduce8(const Acc8& x, float& u0, float& u1) {
  const float t0 = swap32_add(x.a[0][0], x.a[2][0]), t1 = swap32_add(x.a[0][1], x.a[2][1]), t2 = swap32_add(x.a[1][0], x.a[3][0]), t3 = swap32_add(x.a[1][1], x.a[3][1]);
  u0 = swap16_add(t0, t2); u1 = swap16_add(t1, t3);
}
template <int T> struct IC { static constexpr int v = T; };
template <int I, int N, class F> __device__ __forceinline__ void static_for(F&& f) { if constexpr (I < N) { f(IC<I>{}); static_for<I + 1, N>(f); } }
__device__ __forceinline__ void lds_wait_count(float* lds, int idx, unsigned want) {
  for (unsigned spins = 0; spins < SPIN_CAP; ++spins) {
    const unsigned cnt = __hip_atomic_load(reinterpret_cast<unsigned*>(lds) + idx, RLX_WG);
    if (__builtin_amdgcn_readfirstlane(cnt) >= want) break;
    __builtin_amdgcn_s_sleep(1);
  }
  asm volatile("" ::: "memory");
}
__device__ __forceinline__ void lds_arrive(float* lds, int idx, int lane) {
  asm volatile("s_waitcnt lgkmcnt(0)" ::: "memory");
  if (lane == 0) __hip_atomic_fetch_add(reinterpret_cast<unsigned*>(lds) + idx, 1u, RLX_WG);
}
constexpr int cu_issued_before(int n) { return n < 2 * (A - D) ? A + n / 2 + 1 : n + D + 1; }

struct Args {
  const char* x; const char* k; const char* v; const char* wq; const char* wo; const char* wg; const char* wu; const char* wd;
  char* out; char* ws;
};

__global__ void __launch_bounds__(NTHR, 2) fused_decode_kernel(Args a) {
  __shared__ __attribute__((aligned(16))) float lds[L_FLOATS];
  const int tid = threadIdx.x, lane = tid & 63, w = __builtin_amdgcn_readfirstlane(tid >> 6);
  const int g = blockIdx.x, xcd = g & 7;
  const int s = (g >> 3) & 1, hh = (g >> 4) & 3, b = g >> 6, h = hh * 8 + xcd;
  const int r = lane >> 4, c = lane & 15;
  const int pidx = h * 2 + s;
  const int cb = w & 3, rh = w >> 2;
#define STAMP(i) do { } while (0)
#define STAMP_FLUSH(n) do { } while (0)
  STAMP(0);
  unsigned ep;
  asm volatile("global_load_dword %0, %1, %2 sc1" : "=v"(ep) : "v"(0u), "s"(a.ws + WS_EPOCH + b * 256) : "memory");
  const char* wob = a.wo + (size_t)(h * DH) * (DM * 2) + cb * 256;
  const unsigned vow = (unsigned)(r * 16) * (DM * 2) + c * 16;
  Acc8 wf[16];
  unsigned tag;

  if (w < 4) {
    auto level = [&](int sched) -> int {
      const unsigned V = (unsigned)__builtin_amdgcn_s_memrealtime() - (unsigned)sched;
      unsigned* lv = reinterpret_cast<unsigned*>(lds) + L_VT;
      __hip_atomic_store(lv + w, V, RLX_WG);
      const unsigned vb_ = __hip_atomic_load(lv + 4, RLX_WG), ok = __hip_atomic_load(lv + 5, RLX_WG);
      const int d = (int)(V - vb_);
      return __builtin_amdgcn_readfirstlane(ok == 0u ? 1 : (d < -TMARG ? 2 : (d < TMARG ? 1 : 0)));
    };
    const size_t kvoff = (size_t)(b * NH + h) * SK * (DH * 2) + s * 16384 + w * 4096;
    const char* kb = a.k + kvoff;
    const char* vb = a.v + kvoff;
    unsigned vo4[4];
#pragma unroll
    for (int i = 0; i < 4; ++i) vo4[i] = lane * 16 + i * 32768;
    u32x4 win[NUN];
    u32x4 rs[R];
#pragma unroll
    for (int i = 0; i < R; ++i) rs[i] = u32x4{0u, 0u, 0u, 0u};
    static_for<0, A>([&](auto i) {
      constexpr int I = i.v;
      if constexpr (I < DMID) ld16_nt<(I & 3) * 1024>(win[I], vo4[(I & 15) >> 2], kb + (I >> 4) * ITB);
      else pace_ld_nt<DMID - 1, (I & 3) * 1024>(win[I], vo4[(I & 15) >> 2], kb + (I >> 4) * ITB);
    });
    STAMP(1);
    wg_barrier();
    STAMP(2);
    unsigned qh[4], ql[4];
    {
      float q0 = 0.f, q1 = 0.f;
#pragma unroll
      for (int i = 0; i < 4; ++i) { const f32x2 t = *reinterpret_cast<const f32x2*>(&lds[L_QP + i * 128 + 8 * c + 2 * r]); q0 += t[0]; q1 += t[1]; }
      constexpr float QS = 0.08838834764831845f * 1.4426950408889634f;
      q0 *= QS; q1 *= QS;
      const unsigned ph = pack_bf16(q0, q1);
      const unsigned pl = pack_bf16(q0 - lo16(ph), q1 - hi16(ph));
      {
        auto s1 = __builtin_amdgcn_permlane16_swap(ph, ph, false, false);
        auto e = __builtin_amdgcn_permlane32_swap(s1[0], s1[0], false, false);
        auto o = __builtin_amdgcn_permlane32_swap(s1[1], s1[1], false, false);
        qh[0] = e[0]; qh[2] = e[1]; qh[1] = o[0]; qh[3] = o[1];
      }
      {
        auto s1 = __builtin_amdgcn_permlane16_swap(pl, pl, false, false);
        auto e = __builtin_amdgcn_permlane32_swap(s1[0], s1[0], false, false);
        auto o = __builtin_amdgcn_permlane32_swap(s1[1], s1[1], false, false);
        ql[0] = e[0]; ql[2] = e[1]; ql[1] = o[0]; ql[3] = o[1];
      }
    }
    STAMP(3);
    float sc[NIT];
#pragma unroll
    for (int i = 0; i < NIT; ++i) sc[i] = 0.f;
    const bool b0 = (c & 1) != 0, b1 = (c & 2) != 0;
    const int cq = c >> 2;
    auto reduce4 = [&](const float (&p)[4]) -> float {
      const float k0 = b0 ? p[1] : p[0], s0 = b0 ? p[0] : p[1];
      const float k1 = b0 ? p[3] : p[2], s1 = b0 ? p[2] : p[3];
      const float a0 = k0 + dpp<XOR1>(s0), a1 = k1 + dpp<XOR1>(s1);
      const float k2 = b1 ? a1 : a0, s2 = b1 ? a0 : a1;
      float t = k2 + dpp<XOR2>(s2);
      t += dpp<ROR4>(t); t += dpp<ROR8>(t);
      return t;
    };
    auto kdot = [&](const u32x4& kv) -> float {
      float d0 = dot2(kv[0], qh[0], 0.f), d1 = dot2(kv[0], ql[0], 0.f);
#pragma unroll
      for (int k = 1; k < 4; ++k) { d0 = dot2(kv[k], qh[k], d0); d1 = dot2(kv[k], ql[k], d1); }
      return d0 + d1;
    };
    {
      float part[4], snew = 0.f;
      static_for<0, NUN>([&](auto j) {
        constexpr int N = j.v;
        constexpr int I = cu_issued_before(N) - 1;
        constexpr bool issues = (N == 0) || (cu_issued_before(N) != cu_issued_before(N - 1));
        if constexpr (issues) {
          if constexpr (I < NUN) pace_ld_nt<DMID - 1, (I & 3) * 1024>(win[I], vo4[(I & 15) >> 2], kb + (I >> 4) * ITB);
          else wait_ld_nt<DMID - 1, (I & 3) * 1024>(win[N], rs[I % R], vo4[(I & 15) >> 2], kb + (I >> 4) * ITB);
        }
        landed(win[N]);
        part[N & 3] = kdot(win[N]);
        if constexpr ((N & 3) == 3) { const float t = reduce4(part); snew = (cq == ((N & 15) >> 2)) ? t : snew; }
        if constexpr ((N & 15) == 15) sc[NIT - NUN / 16 + N / 16] = snew;
        SBAR();
      });
    }
    for (int it = NUN / 16; it < NIT; ++it) {
      const char* pc = kb + (size_t)it * ITB;
      const char* pn = it < NIT - 1 ? pc + ITB : vb;
      float part[4], snew = 0.f;
      const int lvl = level(TOFF + it * TAUK);
      static_for<0, 16>([&](auto j) {
        constexpr int J = j.v;
        wait_ld_thr<((J + D) & 3) * 1024>(rs[J % R], rs[(J + D) % R], vo4[((J + D) & 15) >> 2], (J + D >= 16) ? pn : pc, lvl);
        part[J & 3] = kdot(rs[J % R]);
        if constexpr ((J & 3) == 3) { const float t = reduce4(part); snew = (cq == (J >> 2)) ? t : snew; }
        SBAR();
      });
#pragma unroll
      for (int i = 0; i < NIT - 1; ++i) sc[i] = sc[i + 1];
      sc[NIT - 1] = snew;
    }
    STAMP(4);
    asm volatile("" : "+v"(ep));
    tag = __builtin_amdgcn_readfirstlane(ep) + 1u;
    float mw;
    {
      float m = sc[0];
#pragma unroll
      for (int i = 1; i < NIT; ++i) m = fmaxf(m, sc[i]);
      mw = wave_max(m);
      float l = 0.f;
#pragma unroll
      for (int i = 0; i < NIT; ++i) { sc[i] = __builtin_amdgcn_exp2f(sc[i] - mw); l += sc[i]; }
      l = wave_sum(l);
      const float lse = mw + __builtin_amdgcn_logf(l);
      if (lane == 0)
        __hip_atomic_store((gu64*)(a.ws + WS_LSE) + (b * NH + h) * 8 + s * 4 + w, ((unsigned long long)tag << 32) | __float_as_uint(lse), RLX_AGENT);
    }
    Acc8 acc; acc_zero(acc);
    float coef = 0.f;
    for (int it = 0; it < NIT - 2; ++it) {
      const char* pc = vb + (size_t)it * ITB;
      const char* pn = pc + ITB;
      const float pcur = sc[0];
      const int lvl = level(TOFF + 16 * TAUK + it * TAUV);
      static_for<0, 16>([&](auto j) {
        constexpr int J = j.v;
        wait_ld_thr<((J + D) & 3) * 1024>(rs[J % R], rs[(J + D) % R], vo4[((J + D) & 15) >> 2], (J + D >= 16) ? pn : pc, lvl);
        fma8(acc, dpp<BCAST + J>(pcur), rs[J % R]);
        SBAR();
      });
#pragma unroll
      for (int i = 0; i < NIT - 1; ++i) sc[i] = sc[i + 1];
    }
    {
      const char* pc = vb + (size_t)(NIT - 2) * ITB;
      const char* pn = pc + ITB;
      const float pcur = sc[0];
      lds_wait_count(lds, L_CNT + 1, 4u);
      if (lane == 0) __hip_atomic_fetch_add(reinterpret_cast<unsigned*>(lds) + L_VT + 6, 1u, RLX_WG);
      const int lvl = level(TOFF + 16 * TAUK + (NIT - 2) * TAUV);
      static_for<0, 16>([&](auto j) {
        constexpr int J = j.v;
        const u32x4 wt = *reinterpret_cast<const u32x4*>(&lds[L_WO + (w * 16 + J) * 256 + lane * 4]);
        wait_ld_thr<((J + D) & 3) * 1024>(rs[J % R], rs[(J + D) % R], vo4[((J + D) & 15) >> 2], (J + D >= 16) ? pn : pc, lvl);
        fma8(acc, dpp<BCAST + J>(pcur), rs[J % R]);
        unpack8(wf[J], wt);
        SBAR();
      });
    }
    {
      u32x2 lg;
      const gu64* lsep = (const gu64*)(a.ws + WS_LSE) + (b * NH + h) * 8 + (c & 7);
      asm volatile("global_load_dwordx2 %0, %1, off sc1" : "=v"(lg) : "v"(lsep) : "memory");
      const char* pc = vb + (size_t)(NIT - 1) * ITB;
      const float pcur = sc[1];
      const int lvl = level(TOFF + 16 * TAUK + (NIT - 1) * TAUV);
      static_for<0, 16>([&](auto j) {
        constexpr int J = j.v;
        if constexpr (J + D < 16) wait_ld_thr<((J + D) & 3) * 1024>(rs[J % R], rs[(J + D) % R], vo4[((J + D) & 15) >> 2], pc, lvl);
        else wait_vm<15 - J>(rs[J % R]);
        if constexpr (J == D + 1) {
          asm volatile("" : "+v"(lg));
          unsigned lv = lg[0], lt = lg[1];
          for (unsigned spins = 0; spins < SPIN_CAP; ++spins) {
            if (__all(lt == tag)) break;
            __builtin_amdgcn_s_sleep(8);
            const unsigned long long v = __hip_atomic_load(lsep, RLX_AGENT);
            lv = (unsigned)v; lt = (unsigned)(v >> 32);
          }
          const float le = __uint_as_float(lv);
          const float M = row_max(le);
          const float L = 0.5f * row_sum(__builtin_amdgcn_exp2f(le - M));
          coef = __builtin_amdgcn_exp2f(mw - M) / L;
        }
        fma8(acc, dpp<BCAST + J>(pcur), rs[J % R]);
        SBAR();
      });
    }
    STAMP(5);
    float u0, u1;
    xrow_reduce8(acc, u0, u1);
    *reinterpret_cast<f32x2*>(&lds[L_RED + w * 128 + 8 * c + 2 * r]) = f32x2{u0 * coef, u1 * coef};
  } else {
    const int hw = w - 4;
    const int cg = hh * 2 + s;
    const int f0 = xcd * 224 + (cg < 7 ? cg : 6) * 32;
    u32x4 xc; unsigned xo0, xo1;
    {
      const char* xb = a.x + b * (DM * 2);
      ld16_nt<0>(xc, lane * 16, xb);
      asm volatile("global_load_ushort %0, %1, %2 nt" : "=v"(xo0) : "v"((unsigned)(lane * 2)), "s"(xb + hw * 256) : "memory");
      asm volatile("global_load_ushort %0, %1, %2 offset:128 nt" : "=v"(xo1) : "v"((unsigned)(lane * 2)), "s"(xb + hw * 256) : "memory");
    }
    const char* wqb = a.wq + (size_t)(hw * 128) * (NH * DH * 2) + h * (DH * 2);
    const unsigned voq = (unsigned)(r * 16) * (NH * DH * 2) + c * 16;
    u32x4 wt[32];
    static_for<0, HD>([&](auto t) { ld16_nt<0>(wt[t.v], voq, wqb + ((t.v >> 4) * 64 + (t.v & 15)) * (NH * DH * 2)); });
    if (tid == 256) { lds[L_CNT] = 0.f; lds[L_CNT + 1] = 0.f; lds[L_VT + 5] = 0.f; lds[L_VT + 6] = 0.f; }
    asm volatile("s_waitcnt vmcnt(%3)" : "+v"(xc), "+v"(xo0), "+v"(xo1) : "i"(HD) : "memory");
    STAMP(1);
    float xn[2];
    {
      float xv[8];
#pragma unroll
      for (int k = 0; k < 4; ++k) { xv[2 * k] = lo16(xc[k]); xv[2 * k + 1] = hi16(xc[k]); }
      float sm = ((xv[0] + xv[1]) + (xv[2] + xv[3])) + ((xv[4] + xv[5]) + (xv[6] + xv[7]));
      const float mean = wave_sum(sm) * (1.0f / DM);
      float sq = 0.f;
#pragma unroll
      for (int k = 0; k < 8; ++k) { const float d = xv[k] - mean; sq = fmaf(d, d, sq); }
      const float var = wave_sum(sq) * (1.0f / DM);
      const float rstd = 1.0f / sqrtf(var + 1e-5f);
      xn[0] = (lo16(xo0) - mean) * rstd;
      xn[1] = (lo16(xo1) - mean) * rstd;
    }
    {
      Acc8 acc; acc_zero(acc);
      SBAR();
      static_for<0, 32>([&](auto t) {
        constexpr int T = t.v, I = T + HD;
        wait_vm<(I <= 32 ? HD - 1 : 31 - T)>(wt[T]);
        if constexpr (I < 32) ld16_nt<0>(wt[I], voq, wqb + ((I >> 4) * 64 + (I & 15)) * (NH * DH * 2));
        fma8(acc, dpp<BCAST + (T & 15)>(xn[T >> 4]), wt[T]);
        SBAR();
      });
      float u0, u1;
      xrow_reduce8(acc, u0, u1);
      *reinterpret_cast<f32x2*>(&lds[L_QP + hw * 128 + 8 * c + 2 * r]) = f32x2{u0, u1};
    }
    STAMP(2);
    wg_barrier();
    STAMP(3);
    {
      Acc8 ag, au; acc_zero(ag); acc_zero(au);
      const unsigned vog = (unsigned)((lane >> 2) * 4) * (FF * 2) + (lane & 3) * 16;
      const size_t sog = (size_t)(hw * 128) * (FF * 2) + f0 * 2;
      static_for<0, 8>([&](auto t) {
        constexpr int T = t.v;
        u32x4 gt, ut;
        ld16_nt<0>(gt, vog, a.wg + sog + ((T >> 2) * 64 + (T & 3)) * (FF * 2));
        ld16_nt<0>(ut, vog, a.wu + sog + ((T >> 2) * 64 + (T & 3)) * (FF * 2));
        const float xb_ = dpp<(T & 3) * 0x55>(xn[T >> 2]);
        wait_vm<1>(gt); fma8(ag, xb_, gt);
        wait_vm<0>(ut); fma8(au, xb_, ut);
        SBAR();
      });
      float t8[8];
#pragma unroll
      for (int k = 0; k < 8; ++k) t8[k] = swap32_add(ag.a[k >> 1][k & 1], au.a[k >> 1][k & 1]);
#pragma unroll
      for (int k = 0; k < 4; ++k) {
        float t = swap16_add(t8[k], t8[k + 4]); t += dpp<ROR4>(t); t += dpp<ROR8>(t);
        if ((lane & 12) == 0) lds[L_GU + ((r >> 1) * 32 + 8 * (lane & 3) + 4 * (r & 1) + k) * 4 + hw] = t;
      }
      lds_arrive(lds, L_CNT, lane);
      u32x4 wd[8];
      const char* wdb = a.wd + (size_t)f0 * (DM * 2) + hw * 256;
      const unsigned vod = (unsigned)(r * 8) * (DM * 2) + c * 16;
      static_for<0, 8>([&](auto t) {
        constexpr int T = t.v;
        if constexpr (T < 2) ld16_nt<(T & 3) * 1024>(wd[T], vod, wdb + (T >> 2) * 4096);
        else pace_ld_nt<1, (T & 3) * 1024>(wd[T], vod, wdb + (T >> 2) * 4096);
      });
      lds_wait_count(lds, L_CNT, 4u);
      const int f = r * 8 + (c & 7);
      const f32x4 g4 = *reinterpret_cast<const f32x4*>(&lds[L_GU + f * 4]), u4 = *reinterpret_cast<const f32x4*>(&lds[L_GU + (32 + f) * 4]);
      const float gs = (g4[0] + g4[1]) + (g4[2] + g4[3]), us = (u4[0] + u4[1]) + (u4[2] + u4[3]);
      const float hv = gs / (1.0f + __expf(-gs)) * us;
      Acc8 ay; acc_zero(ay);
      asm volatile("s_waitcnt vmcnt(0)" : "+v"(wd[0]), "+v"(wd[1]), "+v"(wd[2]), "+v"(wd[3]), "+v"(wd[4]), "+v"(wd[5]), "+v"(wd[6]), "+v"(wd[7]) :: "memory");
      if (cg < 7) static_for<0, 8>([&](auto t) { fma8(ay, dpp<BCAST + t.v>(hv), wd[t.v]); });
      float y0, y1;
      xrow_reduce8(ay, y0, y1);
      *reinterpret_cast<f32x2*>(&lds[L_YF + hw * 128 + 8 * c + 2 * r]) = f32x2{y0, y1};
    }
    STAMP(4);
    {
      static_for<0, 16>([&](auto t) {
        constexpr int T = t.v;
        u32x4 t0, t1;
        ld16_nt<(T & 3) * 1024>(t0, vow, wob + (T >> 2) * 4096);
        ld16_nt<(T & 3) * 1024>(t1, vow, wob + 65536 + (T >> 2) * 4096);
        wait_vm<1>(t0);
        *reinterpret_cast<u32x4*>(&lds[L_WO + (hw * 16 + T) * 256 + lane * 4]) = t0;
        wait_vm<0>(t1);
        unpack8(wf[T], t1);
        SBAR();
      });
      lds_arrive(lds, L_CNT + 1, lane);
    }
    asm volatile("" : "+v"(ep));
    tag = __builtin_amdgcn_readfirstlane(ep) + 1u;
    STAMP(5);
    if (hw == 0) {
      unsigned* lv = reinterpret_cast<unsigned*>(lds) + L_VT;
      unsigned tg4[4];
#pragma unroll
      for (int i = 0; i < 4; ++i) tg4[i] = __hip_atomic_load((const gu32*)(a.ws + WS_EPOCH + i * 256), RLX_AGENT) + 1u;
      gu64* pg = (gu64*)(a.ws + WS_PROG);
      for (unsigned spins = 0; spins < (1u << 16); ++spins) {
        if (__builtin_amdgcn_readfirstlane(__hip_atomic_load(lv + 6, RLX_WG)) != 0u) break;
        const unsigned now = (unsigned)__builtin_amdgcn_s_memrealtime() >> MSH;
        const unsigned v0 = __hip_atomic_load(lv, RLX_WG), v1 = __hip_atomic_load(lv + 1, RLX_WG), v2 = __hip_atomic_load(lv + 2, RLX_WG), v3 = __hip_atomic_load(lv + 3, RLX_WG);
        const unsigned v = v0 + (unsigned)(((int)(v1 - v0) + (int)(v2 - v0) + (int)(v3 - v0)) >> 2);
        if (lane == 0) __hip_atomic_store(pg + (now & 63u) * 256 + g, ((unsigned long long)tag << 32) | v, RLX_AGENT);
        gu64* pm = (gu64*)(a.ws + WS_MEAN);
        if (g == 0) {
          float sd = 0.f, sn = 0.f;
          const gu64* pr = pg + ((now - 1u) & 63u) * 256 + lane;
#pragma unroll
          for (int i = 0; i < 4; ++i) {
            const unsigned long long x = __hip_atomic_load(pr + i * 64, RLX_AGENT);
            const bool ok = (unsigned)(x >> 32) == tg4[i];
            sd += ok ? (float)(int)((unsigned)x - v) : 0.f; sn += ok ? 1.f : 0.f;
          }
          sd = wave_sum(sd); sn = wave_sum(sn);
          if (lane < 32 && sn >= 128.f) __hip_atomic_store(pm + lane * 64, ((unsigned long long)tag << 32) | (v + (unsigned)(int)(sd / sn)), RLX_AGENT);
        }
        const unsigned long long m = __hip_atomic_load(pm + (g & 31) * 64, RLX_AGENT);
        if (lane == 0 && (unsigned)(m >> 32) == tg4[0]) { __hip_atomic_store(lv + 4, (unsigned)m, RLX_WG); __hip_atomic_store(lv + 5, 1u, RLX_WG); }
        __builtin_amdgcn_s_sleep(MSLEEP);
      }
    }
  }
  wg_barrier();
  {
    float ov = 0.f;
#pragma unroll
    for (int i = 0; i < 4; ++i) ov += lds[L_RED + i * 128 + rh * 64 + lane];
    f32x2 yf = *reinterpret_cast<const f32x2*>(&lds[L_YF + cb * 128 + 8 * c + 2 * r]);
    Acc8 ay; acc_zero(ay);
    static_for<0, 16>([&](auto t) {
      const float o1 = dpp<BCAST + t.v>(ov);
      const f32x2 o2 = {o1, o1};
#pragma unroll
      for (int k = 0; k < 4; ++k) ay.a[k] = __builtin_elementwise_fma(o2, wf[t.v].a[k], ay.a[k]);
    });
    float y0, y1;
    xrow_reduce8(ay, y0, y1);
    if (rh == 1) { y0 += yf[0]; y1 += yf[1]; }
    lds[L_QP + w * 64 + lane] = rh ? y0 : y1;
    wg_barrier();
    const float yo = lds[L_QP + (w ^ 4) * 64 + lane];
    const float ym = rh ? y1 : y0;
    const float ys = rh ? (yo + ym) : (ym + yo);
    gu64* gp = (gu64*)(a.ws + WS_G) + ((size_t)(b * 64 + pidx)) * 512 + (cb * 2 + rh) * 64 + lane;
    __hip_atomic_store(gp, ((unsigned long long)tag << 32) | __float_as_uint(ys), RLX_AGENT);
  }
  STAMP(6);
  if (w != 0) { STAMP(7); STAMP_FLUSH(8); return; }
  {
    const gu64* gp = (const gu64*)(a.ws + WS_G) + (size_t)(b * 64 + (lane >> 3)) * 512 + pidx * 8 + (lane & 7);
    constexpr int NG = 8, GS = 8 * 512;
    const int P = pidx * 8 + (lane & 7), pl = P & 63;
    const int col = (P >> 7) * 128 + 8 * (pl & 15) + 2 * (pl >> 4) + ((P >> 6) & 1);
    const float xv = lo16(__builtin_nontemporal_load(reinterpret_cast<const unsigned short*>(a.x) + b * DM + col));
    unsigned val[NG];
    bool ok = false;
    for (unsigned spins = 0; spins < SPIN_CAP; ++spins) {
      if (!ok) {
        bool o = true;
#pragma unroll
        for (int i = 0; i < NG; ++i) { const unsigned long long v = __hip_atomic_load(gp + i * GS, RLX_AGENT); val[i] = (unsigned)v; o &= (unsigned)(v >> 32) == tag; }
        ok = o;
      }
      if (__all(ok)) break;
      __builtin_amdgcn_s_sleep(GSLEEP);
    }
    float t = 0.f;
#pragma unroll
    for (int i = 0; i < NG; ++i) t += __uint_as_float(val[i]);
    t += dpp<ROR8>(t);
    t = swap16_add(t, t);
    t = swap32_add(t, t);
    if (lane < 8) reinterpret_cast<__bf16*>(a.out)[b * DM + col] = (__bf16)(xv + t);
    if (pidx == 0 && lane == 0) __hip_atomic_store((gu32*)(a.ws + WS_EPOCH + b * 256), tag, RLX_AGENT);
  }
  STAMP(7);
  STAMP_FLUSH(8);
}

extern "C" void kernel_launch(void* const* d_in, const int* in_sizes, int n_in, void* d_out, int out_size, void* d_ws, size_t ws_size,
                              hipStream_t stream) {
  Args a;
  a.x = (const char*)d_in[0]; a.k = (const char*)d_in[1]; a.v = (const char*)d_in[2]; a.wq = (const char*)d_in[3]; a.wo = (const char*)d_in[4];
  a.wg = (const char*)d_in[6]; a.wu = (const char*)d_in[7]; a.wd = (const char*)d_in[8];
  a.out = (char*)d_out; a.ws = (char*)d_ws;
  hipLaunchKernelGGL(fused_decode_kernel, dim3(NWG), dim3(NTHR), 0, stream, a);
}
```

```cpp
#include <hip/hip_runtime.h>
#include <hip/hip_bf16.h>
#include <stdint.h>
#include <cstdio>

typedef unsigned u32x4 __attribute__((ext_vector_type(4)));
typedef unsigned u32x2 __attribute__((ext_vector_type(2)));
typedef float f32x2 __attribute__((ext_vector_type(2)));
typedef float f32x4 __attribute__((ext_vector_type(4)));
typedef __attribute__((ext_vector_type(2))) __bf16 bf16x2_t;
#define GAS __attribute__((address_space(1)))
typedef GAS unsigned gu32;
typedef GAS unsigned long long gu64;
#define RLX_AGENT __ATOMIC_RELAXED, __HIP_MEMORY_SCOPE_AGENT
#define RLX_WG __ATOMIC_RELAXED, __HIP_MEMORY_SCOPE_WORKGROUP

#define KD 8
#define KA 50
#define KHD 8
#define DBG_TS 0

constexpr int DM = 512, NH = 32, SK = 8192, DH = 128, FF = 1792;
constexpr int NWG = 256, NTHR = 512, NSW = 8;
#define TAIL 2
#define GSLEEP 4
#define MSLEEP 48
#define MSH 8
#define KDM 6
#define KDL 4
#define TOFF 80
#define TAUK 240
#define TAUV 222
#define TMARG 30
constexpr int D = KD, R = 16, A = KA, HD = KHD;
constexpr int DMID = KDM, DLO = KDL;
constexpr int ITB = 131072;
constexpr int NIT = 16;
constexpr int NUN = ((2 * (A - D) + 15) / 16) * 16;
constexpr unsigned SPIN_CAP = 1u << 20;
static_assert(D < R && NUN <= 16 * (NIT - 1), "schedule");

constexpr size_t WS_EPOCH = 0;
constexpr size_t WS_LSE = 4096;
constexpr size_t WS_MEAN = 32768;
constexpr size_t WS_PROG = 65536;
constexpr size_t WS_G = 262144;
constexpr size_t WS_TS = WS_G + (size_t)4 * 64 * 128 * 64;

constexpr int L_QP = 0;
constexpr int L_RED = 512;
constexpr int L_GU = 1024;
constexpr int L_YF = 1280;
constexpr int L_CNT = 1792;
constexpr int L_VT = 1800;
constexpr int L_WO = 2048;
constexpr int L_FLOATS = 21504;

template <int OFF> __device__ __forceinline__ void ld16_nt(u32x4& d, unsigned voff, const void* sb) {
  asm volatile("global_load_dwordx4 %0, %1, %2 offset:%3 nt" : "=v"(d) : "v"(voff), "s"(sb), "i"(OFF) : "memory");
}
template <int N, int OFF> __device__ __forceinline__ void pace_ld_nt(u32x4& nxt, unsigned voff, const void* sb) {
  asm volatile("s_waitcnt vmcnt(%3)\n\tglobal_load_dwordx4 %0, %1, %2 offset:%4 nt" : "=v"(nxt) : "v"(voff), "s"(sb), "i"(N), "i"(OFF) : "memory");
}
template <int N, int OFF> __device__ __forceinline__ void wait_ld_nt(u32x4& cur, u32x4& nxt, unsigned voff, const void* sb) {
  asm volatile("s_waitcnt vmcnt(%4)\n\tglobal_load_dwordx4 %1, %2, %3 offset:%5 nt" : "+v"(cur), "+v"(nxt) : "v"(voff), "s"(sb), "i"(N), "i"(OFF) : "memory");
}
template <int OFF> __device__ __forceinline__ void wait_ld_thr(u32x4& cur, u32x4& nxt, unsigned voff, const void* sb, int lvl) {
  asm volatile("s_cmp_lt_i32 %4, 1\n\ts_cbranch_scc1 .Lthr%=\n\ts_waitcnt vmcnt(%6)\n\ts_cmp_lt_i32 %4, 2\n\ts_cbranch_scc1 .Lthr%=\n\ts_waitcnt vmcnt(%7)\n.Lthr%=:\n\t"
               "s_waitcnt vmcnt(%5)\n\tglobal_load_dwordx4 %1, %2, %3 offset:%8 nt"
               : "+v"(cur), "+v"(nxt) : "v"(voff), "s"(sb), "s"(lvl), "i"(D - 1), "i"(DMID - 1), "i"(DLO - 1), "i"(OFF) : "memory", "scc");
}
#define SBAR() __builtin_amdgcn_sched_barrier(0)
template <int N> __device__ __forceinline__ void wait_vm(u32x4& r) { asm volatile("s_waitcnt vmcnt(%1)" : "+v"(r) : "i"(N) : "memory"); }
__device__ __forceinline__ void landed(u32x4& r) { asm volatile("" : "+v"(r)); }
__device__ __forceinline__ void wg_barrier() { asm volatile("s_waitcnt lgkmcnt(0)\n\ts_barrier" ::: "memory"); }

template <int CTRL> __device__ __forceinline__ float dpp(float x) {
  return __builtin_bit_cast(float, __builtin_amdgcn_mov_dpp(__builtin_bit_cast(int, x), CTRL, 0xf, 0xf, true));
}
constexpr int XOR1 = 0xB1, XOR2 = 0x4E, HMIR = 0x141, RMIR = 0x140, ROR4 = 0x124, ROR8 = 0x128, BCAST = 0x150;
__device__ __forceinline__ float lo16(unsigned u) { return __builtin_bit_cast(float, u << 16); }
__device__ __forceinline__ float hi16(unsigned u) { return __builtin_bit_cast(float, u & 0xffff0000u); }
__device__ __forceinline__ float dot2(unsigned k, unsigned q, float acc) {
  return __builtin_amdgcn_fdot2_f32_bf16(__builtin_bit_cast(bf16x2_t, k), __builtin_bit_cast(bf16x2_t, q), acc, false);
}
__device__ __forceinline__ unsigned pack_bf16(float lo, float hi) {
  bf16x2_t v = {(__bf16)lo, (__bf16)hi};
  return __builtin_bit_cast(unsigned, v);
}
__device__ __forceinline__ float swap32_add(float a, float b) {
  auto s = __builtin_amdgcn_permlane32_swap(__float_as_uint(a), __float_as_uint(b), false, false);
  return __uint_as_float(s[0]) + __uint_as_float(s[1]);
}
__device__ __forceinline__ float swap16_add(float a, float b) {
  auto s = __builtin_amdgcn_permlane16_swap(__float_as_uint(a), __float_as_uint(b), false, false);
  return __uint_as_float(s[0]) + __uint_as_float(s[1]);
}
__device__ __forceinline__ float swap32_max(float a) {
  auto s = __builtin_amdgcn_permlane32_swap(__float_as_uint(a), __float_as_uint(a), false, false);
  return fmaxf(__uint_as_float(s[0]), __uint_as_float(s[1]));
}
__device__ __forceinline__ float swap16_max(float a) {
  auto s = __builtin_amdgcn_permlane16_swap(__float_as_uint(a), __float_as_uint(a), false, false);
  return fmaxf(__uint_as_float(s[0]), __uint_as_float(s[1]));
}
__device__ __forceinline__ float row_sum(float x) { x += dpp<XOR1>(x); x += dpp<XOR2>(x); x += dpp<HMIR>(x); x += dpp<RMIR>(x); return x; }
__device__ __forceinline__ float row_max(float x) {
  x = fmaxf(x, dpp<XOR1>(x)); x = fmaxf(x, dpp<XOR2>(x)); x = fmaxf(x, dpp<HMIR>(x)); x = fmaxf(x, dpp<RMIR>(x)); return x;
}
__device__ __forceinline__ float wave_sum(float x) { x = row_sum(x); x = swap16_add(x, x); return swap32_add(x, x); }
__device__ __forceinline__ float wave_max(float x) { x = row_max(x); x = swap16_max(x); return swap32_max(x); }
struct Acc8 { f32x2 a[4]; };
__device__ __forceinline__ void acc_zero(Acc8& acc) {
#pragma unroll
  for (int k = 0; k < 4; ++k) acc.a[k] = f32x2{0.f, 0.f};
}
__device__ __forceinline__ void pin(Acc8& acc) { asm volatile("" : "+v"(acc.a[0]), "+v"(acc.a[1]), "+v"(acc.a[2]), "+v"(acc.a[3])); }
__device__ __forceinline__ void fma8(Acc8& acc, float s, const u32x4& w) {
  const f32x2 s2 = {s, s};
#pragma unroll
  for (int k = 0; k < 4; ++k) acc.a[k] = __builtin_elementwise_fma(s2, f32x2{lo16(w[k]), hi16(w[k])}, acc.a[k]);
  pin(acc);
}
__device__ __forceinline__ void unpack8(Acc8& o, const u32x4& w) {
#pragma unroll
  for (int k = 0; k < 4; ++k) o.a[k] = f32x2{lo16(w[k]), hi16(w[k])};
  pin(o);
}
__device__ __forceinline__ void xrow_reduce8(const Acc8& x, float& u0, float& u1) {
  const float t0 = swap32_add(x.a[0][0], x.a[2][0]), t1 = swap32_add(x.a[0][1], x.a[2][1]), t2 = swap32_add(x.a[1][0], x.a[3][0]), t3 = swap32_add(x.a[1][1], x.a[3][1]);
  u0 = swap16_add(t0, t2); u1 = swap16_add(t1, t3);
}
template <int T> struct IC { static constexpr int v = T; };
template <int I, int N, class F> __device__ __forceinline__ void static_for(F&& f) { if constexpr (I < N) { f(IC<I>{}); static_for<I + 1, N>(f); } }
__device__ __forceinline__ void lds_wait_count(float* lds, int idx, unsigned want) {
  for (unsigned spins = 0; spins < SPIN_CAP; ++spins) {
    const unsigned cnt = __hip_atomic_load(reinterpret_cast<unsigned*>(lds) + idx, RLX_WG);
    if (__builtin_amdgcn_readfirstlane(cnt) >= want) break;
    __builtin_amdgcn_s_sleep(1);
  }
  asm volatile("" ::: "memory");
}
__device__ __forceinline__ void lds_arrive(float* lds, int idx, int lane) {
  asm volatile("s_waitcnt lgkmcnt(0)" ::: "memory");
  if (lane == 0) __hip_atomic_fetch_add(reinterpret_cast<unsigned*>(lds) + idx, 1u, RLX_WG);
}
constexpr int cu_issued_before(int n) { return n < 2 * (A - D) ? A + n / 2 + 1 : n + D + 1; }

struct Args {
  const char* x; const char* k; const char* v; const char* wq; const char* wo; const char* wg; const char* wu; const char* wd;
  char* out; char* ws;
};

__global__ void __launch_bounds__(NTHR, 2) fused_decode_kernel(Args a) {
  __shared__ __attribute__((aligned(16))) float lds[L_FLOATS];
  const int tid = threadIdx.x, lane = tid & 63, w = __builtin_amdgcn_readfirstlane(tid >> 6);
  const int g = blockIdx.x, xcd = g & 7;
  const int s = (g >> 3) & 1, hh = (g >> 4) & 3, b = g >> 6, h = hh * 8 + xcd;
  const int r = lane >> 4, c = lane & 15;
  const int pidx = h * 2 + s;
  const int cb = w & 3, rh = w >> 2;
#define STAMP(i) do { } while (0)
#define STAMP_FLUSH(n) do { } while (0)
  STAMP(0);
  unsigned ep;
  asm volatile("global_load_dword %0, %1, %2 sc1" : "=v"(ep) : "v"(0u), "s"(a.ws + WS_EPOCH + b * 256) : "memory");
  const char* wob = a.wo + (size_t)(h * DH) * (DM * 2) + cb * 256;
  const unsigned vow = (unsigned)(r * 16) * (DM * 2) + c * 16;
  Acc8 wf[16];
  unsigned tag;

  if (w < 4) {
    auto level = [&](int sched) -> int {
      const unsigned V = (unsigned)__builtin_amdgcn_s_memrealtime() - (unsigned)sched;
      unsigned* lv = reinterpret_cast<unsigned*>(lds) + L_VT;
      __hip_atomic_store(lv + w, V, RLX_WG);
      const unsigned vb_ = __hip_atomic_load(lv + 4, RLX_WG), ok = __hip_atomic_load(lv + 5, RLX_WG);
      const int d = (int)(V - vb_);
      return __builtin_amdgcn_readfirstlane(ok == 0u ? 1 : (d < -TMARG ? 2 : (d < TMARG ? 1 : 0)));
    };
    const size_t kvoff = (size_t)(b * NH + h) * SK * (DH * 2) + s * 16384 + w * 4096;
    const char* kb = a.k + kvoff;
    const char* vb = a.v + kvoff;
    unsigned vo4[4];
#pragma unroll
    for (int i = 0; i < 4; ++i) vo4[i] = lane * 16 + i * 32768;
    u32x4 win[NUN];
    u32x4 rs[R];
#pragma unroll
    for (int i = 0; i < R; ++i) rs[i] = u32x4{0u, 0u, 0u, 0u};
    static_for<0, A>([&](auto i) {
      constexpr int I = i.v;
      if constexpr (I < DMID) ld16_nt<(I & 3) * 1024>(win[I], vo4[(I & 15) >> 2], kb + (I >> 4) * ITB);
      else pace_ld_nt<DMID - 1, (I & 3) * 1024>(win[I], vo4[(I & 15) >> 2], kb + (I >> 4) * ITB);
    });
    STAMP(1);
    wg_barrier();
    STAMP(2);
    unsigned qh[4], ql[4];
    {
      float q0 = 0.f, q1 = 0.f;
#pragma unroll
      for (int i = 0; i < 4; ++i) { const f32x2 t = *reinterpret_cast<const f32x2*>(&lds[L_QP + i * 128 + 8 * c + 2 * r]); q0 += t[0]; q1 += t[1]; }
      constexpr float QS = 0.08838834764831845f * 1.4426950408889634f;
      q0 *= QS; q1 *= QS;
      const unsigned ph = pack_bf16(q0, q1);
      const unsigned pl = pack_bf16(q0 - lo16(ph), q1 - hi16(ph));
      {
        auto s1 = __builtin_amdgcn_permlane16_swap(ph, ph, false, false);
        auto e = __builtin_amdgcn_permlane32_swap(s1[0], s1[0], false, false);
        auto o = __builtin_amdgcn_permlane32_swap(s1[1], s1[1], false, false);
        qh[0] = e[0]; qh[2] = e[1]; qh[1] = o[0]; qh[3] = o[1];
      }
      {
        auto s1 = __builtin_amdgcn_permlane16_swap(pl, pl, false, false);
        auto e = __builtin_amdgcn_permlane32_swap(s1[0], s1[0], false, false);
        auto o = __builtin_amdgcn_permlane32_swap(s1[1], s1[1], false, false);
        ql[0] = e[0]; ql[2] = e[1]; ql[1] = o[0]; ql[3] = o[1];
      }
    }
    STAMP(3);
    float sc[NIT];
#pragma unroll
    for (int i = 0; i < NIT; ++i) sc[i] = 0.f;
    const bool b0 = (c & 1) != 0, b1 = (c & 2) != 0;
    const int cq = c >> 2;
    auto reduce4 = [&](const float (&p)[4]) -> float {
      const float k0 = b0 ? p[1] : p[0], s0 = b0 ? p[0] : p[1];
      const float k1 = b0 ? p[3] : p[2], s1 = b0 ? p[2] : p[3];
      const float a0 = k0 + dpp<XOR1>(s0), a1 = k1 + dpp<XOR1>(s1);
      const float k2 = b1 ? a1 : a0, s2 = b1 ? a0 : a1;
      float t = k2 + dpp<XOR2>(s2);
      t += dpp<ROR4>(t); t += dpp<ROR8>(t);
      return t;
    };
    auto kdot = [&](const u32x4& kv) -> float {
      float d0 = dot2(kv[0], qh[0], 0.f), d1 = dot2(kv[0], ql[0], 0.f);
#pragma unroll
      for (int k = 1; k < 4; ++k) { d0 = dot2(kv[k], qh[k], d0); d1 = dot2(kv[k], ql[k], d1); }
      return d0 + d1;
    };
    {
      float part[4], snew = 0.f;
      static_for<0, NUN>([&](auto j) {
        constexpr int N = j.v;
        constexpr int I = cu_issued_before(N) - 1;
        constexpr bool issues = (N == 0) || (cu_issued_before(N) != cu_issued_before(N - 1));
        if constexpr (issues) {
          if constexpr (I < NUN) pace_ld_nt<DMID - 1, (I & 3) * 1024>(win[I], vo4[(I & 15) >> 2], kb + (I >> 4) * ITB);
          else wait_ld_nt<DMID - 1, (I & 3) * 1024>(win[N], rs[I % R], vo4[(I & 15) >> 2], kb + (I >> 4) * ITB);
        }
        landed(win[N]);
        part[N & 3] = kdot(win[N]);
        if constexpr ((N & 3) == 3) { const float t = reduce4(part); snew = (cq == ((N & 15) >> 2)) ? t : snew; }
        if constexpr ((N & 15) == 15) sc[NIT - NUN / 16 + N / 16] = snew;
        SBAR();
      });
    }
    for (int it = NUN / 16; it < NIT; ++it) {
      const char* pc = kb + (size_t)it * ITB;
      const char* pn = it < NIT - 1 ? pc + ITB : vb;
      float part[4], snew = 0.f;
      const int lvl = level(TOFF + it * TAUK);
      static_for<0, 16>([&](auto j) {
        constexpr int J = j.v;
        wait_ld_thr<((J + D) & 3) * 1024>(rs[J % R], rs[(J + D) % R], vo4[((J + D) & 15) >> 2], (J + D >= 16) ? pn : pc, lvl);
        part[J & 3] = kdot(rs[J % R]);
        if constexpr ((J & 3) == 3) { const float t = reduce4(part); snew = (cq == (J >> 2)) ? t : snew; }
        SBAR();
      });
#pragma unroll
      for (int i = 0; i < NIT - 1; ++i) sc[i] = sc[i + 1];
      sc[NIT - 1] = snew;
    }
    STAMP(4);
    asm volatile("" : "+v"(ep));
    tag = __builtin_amdgcn_readfirstlane(ep) + 1u;
    float mw;
    {
      float m = sc[0];
#pragma unroll
      for (int i = 1; i < NIT; ++i) m = fmaxf(m, sc[i]);
      mw = wave_max(m);
      float l = 0.f;
#pragma unroll
      for (int i = 0; i < NIT; ++i) { sc[i] = __builtin_amdgcn_exp2f(sc[i] - mw); l += sc[i]; }
      l = wave_sum(l);
      const float lse = mw + __builtin_amdgcn_logf(l);
      if (lane == 0)
        __hip_atomic_store((gu64*)(a.ws + WS_LSE) + (b * NH + h) * 8 + s * 4 + w, ((unsigned long long)tag << 32) | __float_as_uint(lse), RLX_AGENT);
    }
    Acc8 acc; acc_zero(acc);
    float coef = 0.f;
    for (int it = 0; it < NIT - 2; ++it) {
      const char* pc = vb + (size_t)it * ITB;
      const char* pn = pc + ITB;
      const float pcur = sc[0];
      const int lvl = level(TOFF + 16 * TAUK + it * TAUV);
      static_for<0, 16>([&](auto j) {
        constexpr int J = j.v;
        wait_ld_thr<((J + D) & 3) * 1024>(rs[J % R], rs[(J + D) % R], vo4[((J + D) & 15) >> 2], (J + D >= 16) ? pn : pc, lvl);
        fma8(acc, dpp<BCAST + J>(pcur), rs[J % R]);
        SBAR();
      });
#pragma unroll
      for (int i = 0; i < NIT - 1; ++i) sc[i] = sc[i + 1];
    }
    {
      const char* pc = vb + (size_t)(NIT - 2) * ITB;
      const char* pn = pc + ITB;
      const float pcur = sc[0];
      lds_wait_count(lds, L_CNT + 1, 4u);
      if (lane == 0) __hip_atomic_fetch_add(reinterpret_cast<unsigned*>(lds) + L_VT + 6, 1u, RLX_WG);
      const int lvl = level(TOFF + 16 * TAUK + (NIT - 2) * TAUV);
      static_for<0, 16>([&](auto j) {
        constexpr int J = j.v;
        const u32x4 wt = *reinterpret_cast<const u32x4*>(&lds[L_WO + (w * 16 + J) * 256 + lane * 4]);
        wait_ld_thr<((J + D) & 3) * 1024>(rs[J % R], rs[(J + D) % R], vo4[((J + D) & 15) >> 2], (J + D >= 16) ? pn : pc, lvl);
        fma8(acc, dpp<BCAST + J>(pcur), rs[J % R]);
        unpack8(wf[J], wt);
        SBAR();
      });
    }
    {
      u32x2 lg;
      const gu64* lsep = (const gu64*)(a.ws + WS_LSE) + (b * NH + h) * 8 + (c & 7);
      asm volatile("global_load_dwordx2 %0, %1, off sc1" : "=v"(lg) : "v"(lsep) : "memory");
      const char* pc = vb + (size_t)(NIT - 1) * ITB;
      const float pcur = sc[1];
      const int lvl = level(TOFF + 16 * TAUK + (NIT - 1) * TAUV);
      static_for<0, 16>([&](auto j) {
        constexpr int J = j.v;
        if constexpr (J + D < 16) wait_ld_thr<((J + D) & 3) * 1024>(rs[J % R], rs[(J + D) % R], vo4[((J + D) & 15) >> 2], pc, lvl);
        else wait_vm<15 - J>(rs[J % R]);
        if constexpr (J == D + 1) {
          asm volatile("" : "+v"(lg));
          unsigned lv = lg[0], lt = lg[1];
          for (unsigned spins = 0; spins < SPIN_CAP; ++spins) {
            if (__all(lt == tag)) break;
            __builtin_amdgcn_s_sleep(8);
            const unsigned long long v = __hip_atomic_load(lsep, RLX_AGENT);
            lv = (unsigned)v; lt = (unsigned)(v >> 32);
          }
          const float le = __uint_as_float(lv);
          const float M = row_max(le);
          const float L = 0.5f * row_sum(__builtin_amdgcn_exp2f(le - M));
          coef = __builtin_amdgcn_exp2f(mw - M) / L;
        }
        fma8(acc, dpp<BCAST + J>(pcur), rs[J % R]);
        SBAR();
      });
    }
    STAMP(5);
    float u0, u1;
    xrow_reduce8(acc, u0, u1);
    *reinterpret_cast<f32x2*>(&lds[L_RED + w * 128 + 8 * c + 2 * r]) = f32x2{u0 * coef, u1 * coef};
  } else {
    const int hw = w - 4;
    const int cg = hh * 2 + s;
    const int f0 = xcd * 224 + (cg < 7 ? cg : 6) * 32;
    u32x4 xc; unsigned xo0, xo1;
    {
      const char* xb = a.x + b * (DM * 2);
      ld16_nt<0>(xc, lane * 16, xb);
      asm volatile("global_load_ushort %0, %1, %2 nt" : "=v"(xo0) : "v"((unsigned)(lane * 2)), "s"(xb + hw * 256) : "memory");
      asm volatile("global_load_ushort %0, %1, %2 offset:128 nt" : "=v"(xo1) : "v"((unsigned)(lane * 2)), "s"(xb + hw * 256) : "memory");
    }
    const char* wqb = a.wq + (size_t)(hw * 128) * (NH * DH * 2) + h * (DH * 2);
    const unsigned voq = (unsigned)(r * 16) * (NH * DH * 2) + c * 16;
    u32x4 wt[32];
    static_for<0, HD>([&](auto t) { ld16_nt<0>(wt[t.v], voq, wqb + ((t.v >> 4) * 64 + (t.v & 15)) * (NH * DH * 2)); });
    if (tid == 256) { lds[L_CNT] = 0.f; lds[L_CNT + 1] = 0.f; lds[L_VT + 5] = 0.f; lds[L_VT + 6] = 0.f; }
    asm volatile("s_waitcnt vmcnt(%3)" : "+v"(xc), "+v"(xo0), "+v"(xo1) : "i"(HD) : "memory");
    STAMP(1);
    float xn[2];
    {
      float xv[8];
#pragma unroll
      for (int k = 0; k < 4; ++k) { xv[2 * k] = lo16(xc[k]); xv[2 * k + 1] = hi16(xc[k]); }
      float sm = ((xv[0] + xv[1]) + (xv[2] + xv[3])) + ((xv[4] + xv[5]) + (xv[6] + xv[7]));
      const float mean = wave_sum(sm) * (1.0f / DM);
      float sq = 0.f;
#pragma unroll
      for (int k = 0; k < 8; ++k) { const float d = xv[k] - mean; sq = fmaf(d, d, sq); }
      const float var = wave_sum(sq) * (1.0f / DM);
      const float rstd = 1.0f / sqrtf(var + 1e-5f);
      xn[0] = (lo16(xo0) - mean) * rstd;
      xn[1] = (lo16(xo1) - mean) * rstd;
    }
    {
      Acc8 acc; acc_zero(acc);
      SBAR();
      static_for<0, 32>([&](auto t) {
        constexpr int T = t.v, I = T + HD;
        wait_vm<(I <= 32 ? HD - 1 : 31 - T)>(wt[T]);
        if constexpr (I < 32) ld16_nt<0>(wt[I], voq, wqb + ((I >> 4) * 64 + (I & 15)) * (NH * DH * 2));
        fma8(acc, dpp<BCAST + (T & 15)>(xn[T >> 4]), wt[T]);
        SBAR();
      });
      float u0, u1;
      xrow_reduce8(acc, u0, u1);
      *reinterpret_cast<f32x2*>(&lds[L_QP + hw * 128 + 8 * c + 2 * r]) = f32x2{u0, u1};
    }
    STAMP(2);
    wg_barrier();
    STAMP(3);
    {
      Acc8 ag, au; acc_zero(ag); acc_zero(au);
      const unsigned vog = (unsigned)((lane >> 2) * 4) * (FF * 2) + (lane & 3) * 16;
      const size_t sog = (size_t)(hw * 128) * (FF * 2) + f0 * 2;
      static_for<0, 8>([&](auto t) {
        constexpr int T = t.v;
        u32x4 gt, ut;
        ld16_nt<0>(gt, vog, a.wg + sog + ((T >> 2) * 64 + (T & 3)) * (FF * 2));
        ld16_nt<0>(ut, vog, a.wu + sog + ((T >> 2) * 64 + (T & 3)) * (FF * 2));
        const float xb_ = dpp<(T & 3) * 0x55>(xn[T >> 2]);
        wait_vm<1>(gt); fma8(ag, xb_, gt);
        wait_vm<0>(ut); fma8(au, xb_, ut);
        SBAR();
      });
      float t8[8];
#pragma unroll
      for (int k = 0; k < 8; ++k) t8[k] = swap32_add(ag.a[k >> 1][k & 1], au.a[k >> 1][k & 1]);
#pragma unroll
      for (int k = 0; k < 4; ++k) {
        float t = swap16_add(t8[k], t8[k + 4]); t += dpp<ROR4>(t); t += dpp<ROR8>(t);
        if ((lane & 12) == 0) lds[L_GU + ((r >> 1) * 32 + 8 * (lane & 3) + 4 * (r & 1) + k) * 4 + hw] = t;
      }
      lds_arrive(lds, L_CNT, lane);
      u32x4 wd[8];
      const char* wdb = a.wd + (size_t)f0 * (DM * 2) + hw * 256;
      const unsigned vod = (unsigned)(r * 8) * (DM * 2) + c * 16;
      static_for<0, 8>([&](auto t) {
        constexpr int T = t.v;
        if constexpr (T < 2) ld16_nt<(T & 3) * 1024>(wd[T], vod, wdb + (T >> 2) * 4096);
        else pace_ld_nt<1, (T & 3) * 1024>(wd[T], vod, wdb + (T >> 2) * 4096);
      });
      lds_wait_count(lds, L_CNT, 4u);
      const int f = r * 8 + (c & 7);
      const f32x4 g4 = *reinterpret_cast<const f32x4*>(&lds[L_GU + f * 4]), u4 = *reinterpret_cast<const f32x4*>(&lds[L_GU + (32 + f) * 4]);
      const float gs = (g4[0] + g4[1]) + (g4[2] + g4[3]), us = (u4[0] + u4[1]) + (u4[2] + u4[3]);
      const float hv = gs / (1.0f + __expf(-gs)) * us;
      Acc8 ay; acc_zero(ay);
      asm volatile("s_waitcnt vmcnt(0)" : "+v"(wd[0]), "+v"(wd[1]), "+v"(wd[2]), "+v"(wd[3]), "+v"(wd[4]), "+v"(wd[5]), "+v"(wd[6]), "+v"(wd[7]) :: "memory");
      if (cg < 7) static_for<0, 8>([&](auto t) { fma8(ay, dpp<BCAST + t.v>(hv), wd[t.v]); });
      float y0, y1;
      xrow_reduce8(ay, y0, y1);
      *reinterpret_cast<f32x2*>(&lds[L_YF + hw * 128 + 8 * c + 2 * r]) = f32x2{y0, y1};
    }
    STAMP(4);
    {
      static_for<0, 16>([&](auto t) {
        constexpr int T = t.v;
        u32x4 t0, t1;
        ld16_nt<(T & 3) * 1024>(t0, vow, wob + (T >> 2) * 4096);
        ld16_nt<(T & 3) * 1024>(t1, vow, wob + 65536 + (T >> 2) * 4096);
        wait_vm<1>(t0);
        *reinterpret_cast<u32x4*>(&lds[L_WO + (hw * 16 + T) * 256 + lane * 4]) = t0;
        wait_vm<0>(t1);
        unpack8(wf[T], t1);
        SBAR();
      });
      lds_arrive(lds, L_CNT + 1, lane);
    }
    asm volatile("" : "+v"(ep));
    tag = __builtin_amdgcn_readfirstlane(ep) + 1u;
    STAMP(5);
    if (hw == 0) {
      unsigned* lv = reinterpret_cast<unsigned*>(lds) + L_VT;
      unsigned tg4[4];
#pragma unroll
      for (int i = 0; i < 4; ++i) tg4[i] = __hip_atomic_load((const gu32*)(a.ws + WS_EPOCH + i * 256), RLX_AGENT) + 1u;
      gu64* pg = (gu64*)(a.ws + WS_PROG);
      for (unsigned spins = 0; spins < (1u << 16); ++spins) {
        if (__builtin_amdgcn_readfirstlane(__hip_atomic_load(lv + 6, RLX_WG)) != 0u) break;
        const unsigned now = (unsigned)__builtin_amdgcn_s_memrealtime() >> MSH;
        const unsigned v0 = __hip_atomic_load(lv, RLX_WG), v1 = __hip_atomic_load(lv + 1, RLX_WG), v2 = __hip_atomic_load(lv + 2, RLX_WG), v3 = __hip_atomic_load(lv + 3, RLX_WG);
        const unsigned v = v0 + (unsigned)(((int)(v1 - v0) + (int)(v2 - v0) + (int)(v3 - v0)) >> 2);
        if (lane == 0) __hip_atomic_store(pg + (now & 63u) * 256 + g, ((unsigned long long)tag << 32) | v, RLX_AGENT);
        gu64* pm = (gu64*)(a.ws + WS_MEAN);
        if (g == 0) {
          float sd = 0.f, sn = 0.f;
          const gu64* pr = pg + ((now - 1u) & 63u) * 256 + lane;
#pragma unroll
          for (int i = 0; i < 4; ++i) {
            const unsigned long long x = __hip_atomic_load(pr + i * 64, RLX_AGENT);
            const bool ok = (unsigned)(x >> 32) == tg4[i];
            sd += ok ? (float)(int)((unsigned)x - v) : 0.f; sn += ok ? 1.f : 0.f;
          }
          sd = wave_sum(sd); sn = wave_sum(sn);
          if (lane < 32 && sn >= 128.f) __hip_atomic_store(pm + lane * 64, ((unsigned long long)tag << 32) | (v + (unsigned)(int)(sd / sn)), RLX_AGENT);
        }
        const unsigned long long m = __hip_atomic_load(pm + (g & 31) * 64, RLX_AGENT);
        if (lane == 0 && (unsigned)(m >> 32) == tg4[0]) { __hip_atomic_store(lv + 4, (unsigned)m, RLX_WG); __hip_atomic_store(lv + 5, 1u, RLX_WG); }
        __builtin_amdgcn_s_sleep(MSLEEP);
      }
    }
  }
  wg_barrier();
  {
    float ov = 0.f;
#pragma unroll
    for (int i = 0; i < 4; ++i) ov += lds[L_RED + i * 128 + rh * 64 + lane];
    f32x2 yf = *reinterpret_cast<const f32x2*>(&lds[L_YF + cb * 128 + 8 * c + 2 * r]);
    Acc8 ay; acc_zero(ay);
    static_for<0, 16>([&](auto t) {
      const float o1 = dpp<BCAST + t.v>(ov);
      const f32x2 o2 = {o1, o1};
#pragma unroll
      for (int k = 0; k < 4; ++k) ay.a[k] = __builtin_elementwise_fma(o2, wf[t.v].a[k], ay.a[k]);
    });
    float y0, y1;
    xrow_reduce8(ay, y0, y1);
    if (rh == 1) { y0 += yf[0]; y1 += yf[1]; }
    lds[L_QP + w * 64 + lane] = rh ? y0 : y1;
    wg_barrier();
    const float yo = lds[L_QP + (w ^ 4) * 64 + lane];
    const float ym = rh ? y1 : y0;
    const float ys = rh ? (yo + ym) : (ym + yo);
    gu64* gp = (gu64*)(a.ws + WS_G) + ((size_t)(b * 64 + pidx)) * 512 + (cb * 2 + rh) * 64 + lane;
    __hip_atomic_store(gp, ((unsigned long long)tag << 32) | __float_as_uint(ys), RLX_AGENT);
  }
  STAMP(6);
  if (w != 0) { STAMP(7); STAMP_FLUSH(8); return; }
  {
    const gu64* gp = (const gu64*)(a.ws + WS_G) + (size_t)(b * 64 + (lane >> 3)) * 512 + pidx * 8 + (lane & 7);
    constexpr int NG = 8, GS = 8 * 512;
    const int P = pidx * 8 + (lane & 7), pl = P & 63;
    const int col = (P >> 7) * 128 + 8 * (pl & 15) + 2 * (pl >> 4) + ((P >> 6) & 1);
    const float xv = lo16(__builtin_nontemporal_load(reinterpret_cast<const unsigned short*>(a.x) + b * DM + col));
    unsigned val[NG];
    bool ok = false;
    for (unsigned spins = 0; spins < SPIN_CAP; ++spins) {
      if (!ok) {
        bool o = true;
#pragma unroll
        for (int i = 0; i < NG; ++i) { const unsigned long long v = __hip_atomic_load(gp + i * GS, RLX_AGENT); val[i] = (unsigned)v; o &= (unsigned)(v >> 32) == tag; }
        ok = o;
      }
      if (__all(ok)) break;
      __builtin_amdgcn_s_sleep(GSLEEP);
    }
    float t = 0.f;
#pragma unroll
    for (int i = 0; i < NG; ++i) t += __uint_as_float(val[i]);
    t += dpp<ROR8>(t);
    t = swap16_add(t, t);
    t = swap32_add(t, t);
    if (lane < 8) reinterpret_cast<__bf16*>(a.out)[b * DM + col] = (__bf16)(xv + t);
    if (pidx == 0 && lane == 0) __hip_atomic_store((gu32*)(a.ws + WS_EPOCH + b * 256), tag, RLX_AGENT);
  }
  STAMP(7);
  STAMP_FLUSH(8);
}

extern "C" void kernel_launch(void* const* d_in, const int* in_sizes, int n_in, void* d_out, int out_size, void* d_ws, size_t ws_size,
                              hipStream_t stream) {
  for (int i = 0; i < n_in; ++i) fprintf(stderr, "in[%d] = %p (%d)\n", i, d_in[i], in_sizes[i]);
  fprintf(stderr, "out %p ws %p size %zu\n", d_out, d_ws, ws_size);
  { hipPointerAttribute_t at; if (hipPointerGetAttributes(&at, d_in[1]) == hipSuccess) fprintf(stderr, "K attr type %d flags %u managed %d\n", (int)at.type, at.allocationFlags, at.isManaged);
    void* base; size_t sz; if (hipMemGetAddressRange((hipDeviceptr_t*)&base, &sz, d_in[1]) == hipSuccess) fprintf(stderr, "K range base %p size %zu\n", base, sz);
    if (hipMemGetAddressRange((hipDeviceptr_t*)&base, &sz, d_in[2]) == hipSuccess) fprintf(stderr, "V range base %p size %zu\n", base, sz); }
  Args a;
  a.x = (const char*)d_in[0]; a.k = (const char*)d_in[1]; a.v = (const char*)d_in[2]; a.wq = (const char*)d_in[3]; a.wo = (const char*)d_in[4];
  a.wg = (const char*)d_in[6]; a.wu = (const char*)d_in[7]; a.wd = (const char*)d_in[8];
  a.out = (char*)d_out; a.ws = (char*)d_ws;
  hipLaunchKernelGGL(fused_decode_kernel, dim3(NWG), dim3(NTHR), 0, stream, a);
}
```

```cpp
#include <hip/hip_runtime.h>
#include <hip/hip_bf16.h>
#include <stdint.h>

typedef unsigned u32x4 __attribute__((ext_vector_type(4)));
typedef unsigned u32x2 __attribute__((ext_vector_type(2)));
typedef float f32x2 __attribute__((ext_vector_type(2)));
typedef float f32x4 __attribute__((ext_vector_type(4)));
typedef __attribute__((ext_vector_type(2))) __bf16 bf16x2_t;
#define GAS __attribute__((address_space(1)))
typedef GAS unsigned gu32;
typedef GAS unsigned long long gu64;
#define RLX_AGENT __ATOMIC_RELAXED, __HIP_MEMORY_SCOPE_AGENT
#define RLX_WG __ATOMIC_RELAXED, __HIP_MEMORY_SCOPE_WORKGROUP

#define KD 8
#define KA 50
#define KHD 8
#define DBG_TS 0

constexpr int DM = 512, NH = 32, SK = 8192, DH = 128, FF = 1792;
constexpr int NWG = 256, NTHR = 512, NSW = 8;
#define TAIL 2
#define GSLEEP 4
#define MSLEEP 48
#define MSH 8
#define KDM 6
#define KDL 4
#define TOFF 80
#define TAUK 240
#define TAUV 222
#define TMARG 30
constexpr int D = KD, R = 16, A = KA, HD = KHD;
constexpr int DMID = KDM, DLO = KDL;
constexpr int ITB = 131072;
constexpr int NIT = 16;
constexpr int NUN = ((2 * (A - D) + 15) / 16) * 16;
constexpr unsigned SPIN_CAP = 1u << 20;
static_assert(D < R && NUN <= 16 * (NIT - 1), "schedule");

constexpr size_t WS_EPOCH = 0;
constexpr size_t WS_LSE = 4096;
constexpr size_t WS_MEAN = 32768;
constexpr size_t WS_PROG = 65536;
constexpr size_t WS_G = 262144;
constexpr size_t WS_TS = WS_G + (size_t)4 * 64 * 128 * 64;

constexpr int L_QP = 0;
constexpr int L_RED = 512;
constexpr int L_GU = 1024;
constexpr int L_YF = 1280;
constexpr int L_CNT = 1792;
constexpr int L_VT = 1800;
constexpr int L_WO = 2048;
constexpr int L_FLOATS = 21504;

template <int OFF> __device__ __forceinline__ void ld16_nt(u32x4& d, unsigned voff, const void* sb) {
  asm volatile("global_load_dwordx4 %0, %1, %2 offset:%3 nt" : "=v"(d) : "v"(voff), "s"(sb), "i"(OFF) : "memory");
}
template <int N, int OFF> __device__ __forceinline__ void pace_ld_nt(u32x4& nxt, unsigned voff, const void* sb) {
  asm volatile("s_waitcnt vmcnt(%3)\n\tglobal_load_dwordx4 %0, %1, %2 offset:%4 nt" : "=v"(nxt) : "v"(voff), "s"(sb), "i"(N), "i"(OFF) : "memory");
}
template <int N, int OFF> __device__ __forceinline__ void wait_ld_nt(u32x4& cur, u32x4& nxt, unsigned voff, const void* sb) {
  asm volatile("s_waitcnt vmcnt(%4)\n\tglobal_load_dwordx4 %1, %2, %3 offset:%5 nt" : "+v"(cur), "+v"(nxt) : "v"(voff), "s"(sb), "i"(N), "i"(OFF) : "memory");
}
template <int OFF> __device__ __forceinline__ void wait_ld_thr(u32x4& cur, u32x4& nxt, unsigned voff, const void* sb, int lvl) {
  asm volatile("s_cmp_lt_i32 %4, 1\n\ts_cbranch_scc1 .Lthr%=\n\ts_waitcnt vmcnt(%6)\n\ts_cmp_lt_i32 %4, 2\n\ts_cbranch_scc1 .Lthr%=\n\ts_waitcnt vmcnt(%7)\n.Lthr%=:\n\t"
               "s_waitcnt vmcnt(%5)\n\tglobal_load_dwordx4 %1, %2, %3 offset:%8 nt"
               : "+v"(cur), "+v"(nxt) : "v"(voff), "s"(sb), "s"(lvl), "i"(D - 1), "i"(DMID - 1), "i"(DLO - 1), "i"(OFF) : "memory", "scc");
}
#define VPOL ""
template <int OFF> __device__ __forceinline__ void wait_ld_thrv(u32x4& cur, u32x4& nxt, unsigned voff, const void* sb, int lvl) {
  asm volatile("s_cmp_lt_i32 %4, 1\n\ts_cbranch_scc1 .Lthr%=\n\ts_waitcnt vmcnt(%6)\n\ts_cmp_lt_i32 %4, 2\n\ts_cbranch_scc1 .Lthr%=\n\ts_waitcnt vmcnt(%7)\n.Lthr%=:\n\t"
               "s_waitcnt vmcnt(%5)\n\tglobal_load_dwordx4 %1, %2, %3 offset:%8 " VPOL
               : "+v"(cur), "+v"(nxt) : "v"(voff), "s"(sb), "s"(lvl), "i"(D - 1), "i"(DMID - 1), "i"(DLO - 1), "i"(OFF) : "memory", "scc");
}
#define SBAR() __builtin_amdgcn_sched_barrier(0)
template <int N> __device__ __forceinline__ void wait_vm(u32x4& r) { asm volatile("s_waitcnt vmcnt(%1)" : "+v"(r) : "i"(N) : "memory"); }
__device__ __forceinline__ void landed(u32x4& r) { asm volatile("" : "+v"(r)); }
__device__ __forceinline__ void wg_barrier() { asm volatile("s_waitcnt lgkmcnt(0)\n\ts_barrier" ::: "memory"); }

template <int CTRL> __device__ __forceinline__ float dpp(float x) {
  return __builtin_bit_cast(float, __builtin_amdgcn_mov_dpp(__builtin_bit_cast(int, x), CTRL, 0xf, 0xf, true));
}
constexpr int XOR1 = 0xB1, XOR2 = 0x4E, HMIR = 0x141, RMIR = 0x140, ROR4 = 0x124, ROR8 = 0x128, BCAST = 0x150;
__device__ __forceinline__ float lo16(unsigned u) { return __builtin_bit_cast(float, u << 16); }
__device__ __forceinline__ float hi16(unsigned u) { return __builtin_bit_cast(float, u & 0xffff0000u); }
__device__ __forceinline__ float dot2(unsigned k, unsigned q, float acc) {
  return __builtin_amdgcn_fdot2_f32_bf16(__builtin_bit_cast(bf16x2_t, k), __builtin_bit_cast(bf16x2_t, q), acc, false);
}
__device__ __forceinline__ unsigned pack_bf16(float lo, float hi) {
  bf16x2_t v = {(__bf16)lo, (__bf16)hi};
  return __builtin_bit_cast(unsigned, v);
}
__device__ __forceinline__ float swap32_add(float a, float b) {
  auto s = __builtin_amdgcn_permlane32_swap(__float_as_uint(a), __float_as_uint(b), false, false);
  return __uint_as_float(s[0]) + __uint_as_float(s[1]);
}
__device__ __forceinline__ float swap16_add(float a, float b) {
  auto s = __builtin_amdgcn_permlane16_swap(__float_as_uint(a), __float_as_uint(b), false, false);
  return __uint_as_float(s[0]) + __uint_as_float(s[1]);
}
__device__ __forceinline__ float swap32_max(float a) {
  auto s = __builtin_amdgcn_permlane32_swap(__float_as_uint(a), __float_as_uint(a), false, false);
  return fmaxf(__uint_as_float(s[0]), __uint_as_float(s[1]));
}
__device__ __forceinline__ float swap16_max(float a) {
  auto s = __builtin_amdgcn_permlane16_swap(__float_as_uint(a), __float_as_uint(a), false, false);
  return fmaxf(__uint_as_float(s[0]), __uint_as_float(s[1]));
}
__device__ __forceinline__ float row_sum(float x) { x += dpp<XOR1>(x); x += dpp<XOR2>(x); x += dpp<HMIR>(x); x += dpp<RMIR>(x); return x; }
__device__ __forceinline__ float row_max(float x) {
  x = fmaxf(x, dpp<XOR1>(x)); x = fmaxf(x, dpp<XOR2>(x)); x = fmaxf(x, dpp<HMIR>(x)); x = fmaxf(x, dpp<RMIR>(x)); return x;
}
__device__ __forceinline__ float wave_sum(float x) { x = row_sum(x); x = swap16_add(x, x); return swap32_add(x, x); }
__device__ __forceinline__ float wave_max(float x) { x = row_max(x); x = swap16_max(x); return swap32_max(x); }
struct Acc8 { f32x2 a[4]; };
__device__ __forceinline__ void acc_zero(Acc8& acc) {
#pragma unroll
  for (int k = 0; k < 4; ++k) acc.a[k] = f32x2{0.f, 0.f};
}
__device__ __forceinline__ void pin(Acc8& acc) { asm volatile("" : "+v"(acc.a[0]), "+v"(acc.a[1]), "+v"(acc.a[2]), "+v"(acc.a[3])); }
__device__ __forceinline__ void fma8(Acc8& acc, float s, const u32x4& w) {
  const f32x2 s2 = {s, s};
#pragma unroll
  for (int k = 0; k < 4; ++k) acc.a[k] = __builtin_elementwise_fma(s2, f32x2{lo16(w[k]), hi16(w[k])}, acc.a[k]);
  pin(acc);
}
__device__ __forceinline__ void unpack8(Acc8& o, const u32x4& w) {
#pragma unroll
  for (int k = 0; k < 4; ++k) o.a[k] = f32x2{lo16(w[k]), hi16(w[k])};
  pin(o);
}
__device__ __forceinline__ void xrow_reduce8(const Acc8& x, float& u0, float& u1) {
  const float t0 = swap32_add(x.a[0][0], x.a[2][0]), t1 = swap32_add(x.a[0][1], x.a[2][1]), t2 = swap32_add(x.a[1][0], x.a[3][0]), t3 = swap32_add(x.a[1][1], x.a[3][1]);
  u0 = swap16_add(t0, t2); u1 = swap16_add(t1, t3);
}
template <int T> struct IC { static constexpr int v = T; };
template <int I, int N, class F> __device__ __forceinline__ void static_for(F&& f) { if constexpr (I < N) { f(IC<I>{}); static_for<I + 1, N>(f); } }
__device__ __forceinline__ void lds_wait_count(float* lds, int idx, unsigned want) {
  for (unsigned spins = 0; spins < SPIN_CAP; ++spins) {
    const unsigned cnt = __hip_atomic_load(reinterpret_cast<unsigned*>(lds) + idx, RLX_WG);
    if (__builtin_amdgcn_readfirstlane(cnt) >= want) break;
    __builtin_amdgcn_s_sleep(1);
  }
  asm volatile("" ::: "memory");
}
__device__ __forceinline__ void lds_arrive(float* lds, int idx, int lane) {
  asm volatile("s_waitcnt lgkmcnt(0)" ::: "memory");
  if (lane == 0) __hip_atomic_fetch_add(reinterpret_cast<unsigned*>(lds) + idx, 1u, RLX_WG);
}
constexpr int cu_issued_before(int n) { return n < 2 * (A - D) ? A + n / 2 + 1 : n + D + 1; }

struct Args {
  const char* x; const char* k; const char* v; const char* wq; const char* wo; const char* wg; const char* wu; const char* wd;
  char* out; char* ws;
};

__global__ void __launch_bounds__(NTHR, 2) fused_decode_kernel(Args a) {
  __shared__ __attribute__((aligned(16))) float lds[L_FLOATS];
  const int tid = threadIdx.x, lane = tid & 63, w = __builtin_amdgcn_readfirstlane(tid >> 6);
  const int g = blockIdx.x, xcd = g & 7;
  const int s = (g >> 3) & 1, hh = (g >> 4) & 3, b = g >> 6, h = hh * 8 + xcd;
  const int r = lane >> 4, c = lane & 15;
  const int pidx = h * 2 + s;
  const int cb = w & 3, rh = w >> 2;
#define STAMP(i) do { } while (0)
#define STAMP_FLUSH(n) do { } while (0)
  STAMP(0);
  unsigned ep;
  asm volatile("global_load_dword %0, %1, %2 sc1" : "=v"(ep) : "v"(0u), "s"(a.ws + WS_EPOCH + b * 256) : "memory");
  const char* wob = a.wo + (size_t)(h * DH) * (DM * 2) + cb * 256;
  const unsigned vow = (unsigned)(r * 16) * (DM * 2) + c * 16;
  Acc8 wf[16];
  unsigned tag;

  if (w < 4) {
    auto level = [&](int sched) -> int {
      const unsigned V = (unsigned)__builtin_amdgcn_s_memrealtime() - (unsigned)sched;
      unsigned* lv = reinterpret_cast<unsigned*>(lds) + L_VT;
      __hip_atomic_store(lv + w, V, RLX_WG);
      const unsigned vb_ = __hip_atomic_load(lv + 4, RLX_WG), ok = __hip_atomic_load(lv + 5, RLX_WG);
      const int d = (int)(V - vb_);
      return __builtin_amdgcn_readfirstlane(ok == 0u ? 1 : (d < -TMARG ? 2 : (d < TMARG ? 1 : 0)));
    };
    const size_t kvoff = (size_t)(b * NH + h) * SK * (DH * 2) + s * 16384 + w * 4096;
    const char* kb = a.k + kvoff;
    const char* vb = a.v + kvoff;
    unsigned vo4[4];
#pragma unroll
    for (int i = 0; i < 4; ++i) vo4[i] = lane * 16 + i * 32768;
    u32x4 win[NUN];
    u32x4 rs[R];
#pragma unroll
    for (int i = 0; i < R; ++i) rs[i] = u32x4{0u, 0u, 0u, 0u};
    static_for<0, A>([&](auto i) {
      constexpr int I = i.v;
      if constexpr (I < DMID) ld16_nt<(I & 3) * 1024>(win[I], vo4[(I & 15) >> 2], kb + (I >> 4) * ITB);
      else pace_ld_nt<DMID - 1, (I & 3) * 1024>(win[I], vo4[(I & 15) >> 2], kb + (I >> 4) * ITB);
    });
    STAMP(1);
    wg_barrier();
    STAMP(2);
    unsigned qh[4], ql[4];
    {
      float q0 = 0.f, q1 = 0.f;
#pragma unroll
      for (int i = 0; i < 4; ++i) { const f32x2 t = *reinterpret_cast<const f32x2*>(&lds[L_QP + i * 128 + 8 * c + 2 * r]); q0 += t[0]; q1 += t[1]; }
      constexpr float QS = 0.08838834764831845f * 1.4426950408889634f;
      q0 *= QS; q1 *= QS;
      const unsigned ph = pack_bf16(q0, q1);
      const unsigned pl = pack_bf16(q0 - lo16(ph), q1 - hi16(ph));
      {
        auto s1 = __builtin_amdgcn_permlane16_swap(ph, ph, false, false);
        auto e = __builtin_amdgcn_permlane32_swap(s1[0], s1[0], false, false);
        auto o = __builtin_amdgcn_permlane32_swap(s1[1], s1[1], false, false);
        qh[0] = e[0]; qh[2] = e[1]; qh[1] = o[0]; qh[3] = o[1];
      }
      {
        auto s1 = __builtin_amdgcn_permlane16_swap(pl, pl, false, false);
        auto e = __builtin_amdgcn_permlane32_swap(s1[0], s1[0], false, false);
        auto o = __builtin_amdgcn_permlane32_swap(s1[1], s1[1], false, false);
        ql[0] = e[0]; ql[2] = e[1]; ql[1] = o[0]; ql[3] = o[1];
      }
    }
    STAMP(3);
    float sc[NIT];
#pragma unroll
    for (int i = 0; i < NIT; ++i) sc[i] = 0.f;
    const bool b0 = (c & 1) != 0, b1 = (c & 2) != 0;
    const int cq = c >> 2;
    auto reduce4 = [&](const float (&p)[4]) -> float {
      const float k0 = b0 ? p[1] : p[0], s0 = b0 ? p[0] : p[1];
      const float k1 = b0 ? p[3] : p[2], s1 = b0 ? p[2] : p[3];
      const float a0 = k0 + dpp<XOR1>(s0), a1 = k1 + dpp<XOR1>(s1);
      const float k2 = b1 ? a1 : a0, s2 = b1 ? a0 : a1;
      float t = k2 + dpp<XOR2>(s2);
      t += dpp<ROR4>(t); t += dpp<ROR8>(t);
      return t;
    };
    auto kdot = [&](const u32x4& kv) -> float {
      float d0 = dot2(kv[0], qh[0], 0.f), d1 = dot2(kv[0], ql[0], 0.f);
#pragma unroll
      for (int k = 1; k < 4; ++k) { d0 = dot2(kv[k], qh[k], d0); d1 = dot2(kv[k], ql[k], d1); }
      return d0 + d1;
    };
    {
      float part[4], snew = 0.f;
      static_for<0, NUN>([&](auto j) {
        constexpr int N = j.v;
        constexpr int I = cu_issued_before(N) - 1;
        constexpr bool issues = (N == 0) || (cu_issued_before(N) != cu_issued_before(N - 1));
        if constexpr (issues) {
          if constexpr (I < NUN) pace_ld_nt<DMID - 1, (I & 3) * 1024>(win[I], vo4[(I & 15) >> 2], kb + (I >> 4) * ITB);
          else wait_ld_nt<DMID - 1, (I & 3) * 1024>(win[N], rs[I % R], vo4[(I & 15) >> 2], kb + (I >> 4) * ITB);
        }
        landed(win[N]);
        part[N & 3] = kdot(win[N]);
        if constexpr ((N & 3) == 3) { const float t = reduce4(part); snew = (cq == ((N & 15) >> 2)) ? t : snew; }
        if constexpr ((N & 15) == 15) sc[NIT - NUN / 16 + N / 16] = snew;
        SBAR();
      });
    }
    for (int it = NUN / 16; it < NIT; ++it) {
      const char* pc = kb + (size_t)it * ITB;
      const char* pn = it < NIT - 1 ? pc + ITB : vb;
      float part[4], snew = 0.f;
      const int lvl = level(TOFF + it * TAUK);
      static_for<0, 16>([&](auto j) {
        constexpr int J = j.v;
        wait_ld_thr<((J + D) & 3) * 1024>(rs[J % R], rs[(J + D) % R], vo4[((J + D) & 15) >> 2], (J + D >= 16) ? pn : pc, lvl);
        part[J & 3] = kdot(rs[J % R]);
        if constexpr ((J & 3) == 3) { const float t = reduce4(part); snew = (cq == (J >> 2)) ? t : snew; }
        SBAR();
      });
#pragma unroll
      for (int i = 0; i < NIT - 1; ++i) sc[i] = sc[i + 1];
      sc[NIT - 1] = snew;
    }
    STAMP(4);
    asm volatile("" : "+v"(ep));
    tag = __builtin_amdgcn_readfirstlane(ep) + 1u;
    float mw;
    {
      float m = sc[0];
#pragma unroll
      for (int i = 1; i < NIT; ++i) m = fmaxf(m, sc[i]);
      mw = wave_max(m);
      float l = 0.f;
#pragma unroll
      for (int i = 0; i < NIT; ++i) { sc[i] = __builtin_amdgcn_exp2f(sc[i] - mw); l += sc[i]; }
      l = wave_sum(l);
      const float lse = mw + __builtin_amdgcn_logf(l);
      if (lane == 0)
        __hip_atomic_store((gu64*)(a.ws + WS_LSE) + (b * NH + h) * 8 + s * 4 + w, ((unsigned long long)tag << 32) | __float_as_uint(lse), RLX_AGENT);
    }
    Acc8 acc; acc_zero(acc);
    float coef = 0.f;
    for (int it = 0; it < NIT - 2; ++it) {
      const char* pc = vb + (size_t)it * ITB;
      const char* pn = pc + ITB;
      const float pcur = sc[0];
      const int lvl = level(TOFF + 16 * TAUK + it * TAUV);
      static_for<0, 16>([&](auto j) {
        constexpr int J = j.v;
        wait_ld_thrv<((J + D) & 3) * 1024>(rs[J % R], rs[(J + D) % R], vo4[((J + D) & 15) >> 2], (J + D >= 16) ? pn : pc, lvl);
        fma8(acc, dpp<BCAST + J>(pcur), rs[J % R]);
        SBAR();
      });
#pragma unroll
      for (int i = 0; i < NIT - 1; ++i) sc[i] = sc[i + 1];
    }
    {
      const char* pc = vb + (size_t)(NIT - 2) * ITB;
      const char* pn = pc + ITB;
      const float pcur = sc[0];
      lds_wait_count(lds, L_CNT + 1, 4u);
      if (lane == 0) __hip_atomic_fetch_add(reinterpret_cast<unsigned*>(lds) + L_VT + 6, 1u, RLX_WG);
      const int lvl = level(TOFF + 16 * TAUK + (NIT - 2) * TAUV);
      static_for<0, 16>([&](auto j) {
        constexpr int J = j.v;
        const u32x4 wt = *reinterpret_cast<const u32x4*>(&lds[L_WO + (w * 16 + J) * 256 + lane * 4]);
        wait_ld_thrv<((J + D) & 3) * 1024>(rs[J % R], rs[(J + D) % R], vo4[((J + D) & 15) >> 2], (J + D >= 16) ? pn : pc, lvl);
        fma8(acc, dpp<BCAST + J>(pcur), rs[J % R]);
        unpack8(wf[J], wt);
        SBAR();
      });
    }
    {
      u32x2 lg;
      const gu64* lsep = (const gu64*)(a.ws + WS_LSE) + (b * NH + h) * 8 + (c & 7);
      asm volatile("global_load_dwordx2 %0, %1, off sc1" : "=v"(lg) : "v"(lsep) : "memory");
      const char* pc = vb + (size_t)(NIT - 1) * ITB;
      const float pcur = sc[1];
      const int lvl = level(TOFF + 16 * TAUK + (NIT - 1) * TAUV);
      static_for<0, 16>([&](auto j) {
        constexpr int J = j.v;
        if constexpr (J + D < 16) wait_ld_thrv<((J + D) & 3) * 1024>(rs[J % R], rs[(J + D) % R], vo4[((J + D) & 15) >> 2], pc, lvl);
        else wait_vm<15 - J>(rs[J % R]);
        if constexpr (J == D + 1) {
          asm volatile("" : "+v"(lg));
          unsigned lv = lg[0], lt = lg[1];
          for (unsigned spins = 0; spins < SPIN_CAP; ++spins) {
            if (__all(lt == tag)) break;
            __builtin_amdgcn_s_sleep(8);
            const unsigned long long v = __hip_atomic_load(lsep, RLX_AGENT);
            lv = (unsigned)v; lt = (unsigned)(v >> 32);
          }
          const float le = __uint_as_float(lv);
          const float M = row_max(le);
          const float L = 0.5f * row_sum(__builtin_amdgcn_exp2f(le - M));
          coef = __builtin_amdgcn_exp2f(mw - M) / L;
        }
        fma8(acc, dpp<BCAST + J>(pcur), rs[J % R]);
        SBAR();
      });
    }
    STAMP(5);
    float u0, u1;
    xrow_reduce8(acc, u0, u1);
    *reinterpret_cast<f32x2*>(&lds[L_RED + w * 128 + 8 * c + 2 * r]) = f32x2{u0 * coef, u1 * coef};
  } else {
    const int hw = w - 4;
    const int cg = hh * 2 + s;
    const int f0 = xcd * 224 + (cg < 7 ? cg : 6) * 32;
    u32x4 xc; unsigned xo0, xo1;
    {
      const char* xb = a.x + b * (DM * 2);
      ld16_nt<0>(xc, lane * 16, xb);
      asm volatile("global_load_ushort %0, %1, %2 nt" : "=v"(xo0) : "v"((unsigned)(lane * 2)), "s"(xb + hw * 256) : "memory");
      asm volatile("global_load_ushort %0, %1, %2 offset:128 nt" : "=v"(xo1) : "v"((unsigned)(lane * 2)), "s"(xb + hw * 256) : "memory");
    }
    const char* wqb = a.wq + (size_t)(hw * 128) * (NH * DH * 2) + h * (DH * 2);
    const unsigned voq = (unsigned)(r * 16) * (NH * DH * 2) + c * 16;
    u32x4 wt[32];
    static_for<0, HD>([&](auto t) { ld16_nt<0>(wt[t.v], voq, wqb + ((t.v >> 4) * 64 + (t.v & 15)) * (NH * DH * 2)); });
    if (tid == 256) { lds[L_CNT] = 0.f; lds[L_CNT + 1] = 0.f; lds[L_VT + 5] = 0.f; lds[L_VT + 6] = 0.f; }
    asm volatile("s_waitcnt vmcnt(%3)" : "+v"(xc), "+v"(xo0), "+v"(xo1) : "i"(HD) : "memory");
    STAMP(1);
    float xn[2];
    {
      float xv[8];
#pragma unroll
      for (int k = 0; k < 4; ++k) { xv[2 * k] = lo16(xc[k]); xv[2 * k + 1] = hi16(xc[k]); }
      float sm = ((xv[0] + xv[1]) + (xv[2] + xv[3])) + ((xv[4] + xv[5]) + (xv[6] + xv[7]));
      const float mean = wave_sum(sm) * (1.0f / DM);
      float sq = 0.f;
#pragma unroll
      for (int k = 0; k < 8; ++k) { const float d = xv[k] - mean; sq = fmaf(d, d, sq); }
      const float var = wave_sum(sq) * (1.0f / DM);
      const float rstd = 1.0f / sqrtf(var + 1e-5f);
      xn[0] = (lo16(xo0) - mean) * rstd;
      xn[1] = (lo16(xo1) - mean) * rstd;
    }
    {
      Acc8 acc; acc_zero(acc);
      SBAR();
      static_for<0, 32>([&](auto t) {
        constexpr int T = t.v, I = T + HD;
        wait_vm<(I <= 32 ? HD - 1 : 31 - T)>(wt[T]);
        if constexpr (I < 32) ld16_nt<0>(wt[I], voq, wqb + ((I >> 4) * 64 + (I & 15)) * (NH * DH * 2));
        fma8(acc, dpp<BCAST + (T & 15)>(xn[T >> 4]), wt[T]);
        SBAR();
      });
      float u0, u1;
      xrow_reduce8(acc, u0, u1);
      *reinterpret_cast<f32x2*>(&lds[L_QP + hw * 128 + 8 * c + 2 * r]) = f32x2{u0, u1};
    }
    STAMP(2);
    wg_barrier();
    STAMP(3);
    {
      Acc8 ag, au; acc_zero(ag); acc_zero(au);
      const unsigned vog = (unsigned)((lane >> 2) * 4) * (FF * 2) + (lane & 3) * 16;
      const size_t sog = (size_t)(hw * 128) * (FF * 2) + f0 * 2;
      static_for<0, 8>([&](auto t) {
        constexpr int T = t.v;
        u32x4 gt, ut;
        ld16_nt<0>(gt, vog, a.wg + sog + ((T >> 2) * 64 + (T & 3)) * (FF * 2));
        ld16_nt<0>(ut, vog, a.wu + sog + ((T >> 2) * 64 + (T & 3)) * (FF * 2));
        const float xb_ = dpp<(T & 3) * 0x55>(xn[T >> 2]);
        wait_vm<1>(gt); fma8(ag, xb_, gt);
        wait_vm<0>(ut); fma8(au, xb_, ut);
        SBAR();
      });
      float t8[8];
#pragma unroll
      for (int k = 0; k < 8; ++k) t8[k] = swap32_add(ag.a[k >> 1][k & 1], au.a[k >> 1][k & 1]);
#pragma unroll
      for (int k = 0; k < 4; ++k) {
        float t = swap16_add(t8[k], t8[k + 4]); t += dpp<ROR4>(t); t += dpp<ROR8>(t);
        if ((lane & 12) == 0) lds[L_GU + ((r >> 1) * 32 + 8 * (lane & 3) + 4 * (r & 1) + k) * 4 + hw] = t;
      }
      lds_arrive(lds, L_CNT, lane);
      u32x4 wd[8];
      const char* wdb = a.wd + (size_t)f0 * (DM * 2) + hw * 256;
      const unsigned vod = (unsigned)(r * 8) * (DM * 2) + c * 16;
      static_for<0, 8>([&](auto t) {
        constexpr int T = t.v;
        if constexpr (T < 2) ld16_nt<(T & 3) * 1024>(wd[T], vod, wdb + (T >> 2) * 4096);
        else pace_ld_nt<1, (T & 3) * 1024>(wd[T], vod, wdb + (T >> 2) * 4096);
      });
      lds_wait_count(lds, L_CNT, 4u);
      const int f = r * 8 + (c & 7);
      const f32x4 g4 = *reinterpret_cast<const f32x4*>(&lds[L_GU + f * 4]), u4 = *reinterpret_cast<const f32x4*>(&lds[L_GU + (32 + f) * 4]);
      const float gs = (g4[0] + g4[1]) + (g4[2] + g4[3]), us = (u4[0] + u4[1]) + (u4[2] + u4[3]);
      const float hv = gs / (1.0f + __expf(-gs)) * us;
      Acc8 ay; acc_zero(ay);
      asm volatile("s_waitcnt vmcnt(0)" : "+v"(wd[0]), "+v"(wd[1]), "+v"(wd[2]), "+v"(wd[3]), "+v"(wd[4]), "+v"(wd[5]), "+v"(wd[6]), "+v"(wd[7]) :: "memory");
      if (cg < 7) static_for<0, 8>([&](auto t) { fma8(ay, dpp<BCAST + t.v>(hv), wd[t.v]); });
      float y0, y1;
      xrow_reduce8(ay, y0, y1);
      *reinterpret_cast<f32x2*>(&lds[L_YF + hw * 128 + 8 * c + 2 * r]) = f32x2{y0, y1};
    }
    STAMP(4);
    {
      static_for<0, 16>([&](auto t) {
        constexpr int T = t.v;
        u32x4 t0, t1;
        ld16_nt<(T & 3) * 1024>(t0, vow, wob + (T >> 2) * 4096);
        ld16_nt<(T & 3) * 1024>(t1, vow, wob + 65536 + (T >> 2) * 4096);
        wait_vm<1>(t0);
        *reinterpret_cast<u32x4*>(&lds[L_WO + (hw * 16 + T) * 256 + lane * 4]) = t0;
        wait_vm<0>(t1);
        unpack8(wf[T], t1);
        SBAR();
      });
      lds_arrive(lds, L_CNT + 1, lane);
    }
    asm volatile("" : "+v"(ep));
    tag = __builtin_amdgcn_readfirstlane(ep) + 1u;
    STAMP(5);
    if (hw == 0) {
      unsigned* lv = reinterpret_cast<unsigned*>(lds) + L_VT;
      unsigned tg4[4];
#pragma unroll
      for (int i = 0; i < 4; ++i) tg4[i] = __hip_atomic_load((const gu32*)(a.ws + WS_EPOCH + i * 256), RLX_AGENT) + 1u;
      gu64* pg = (gu64*)(a.ws + WS_PROG);
      for (unsigned spins = 0; spins < (1u << 16); ++spins) {
        if (__builtin_amdgcn_readfirstlane(__hip_atomic_load(lv + 6, RLX_WG)) != 0u) break;
        const unsigned now = (unsigned)__builtin_amdgcn_s_memrealtime() >> MSH;
        const unsigned v0 = __hip_atomic_load(lv, RLX_WG), v1 = __hip_atomic_load(lv + 1, RLX_WG), v2 = __hip_atomic_load(lv + 2, RLX_WG), v3 = __hip_atomic_load(lv + 3, RLX_WG);
        const unsigned v = v0 + (unsigned)(((int)(v1 - v0) + (int)(v2 - v0) + (int)(v3 - v0)) >> 2);
        if (lane == 0) __hip_atomic_store(pg + (now & 63u) * 256 + g, ((unsigned long long)tag << 32) | v, RLX_AGENT);
        gu64* pm = (gu64*)(a.ws + WS_MEAN);
        if (g == 0) {
          float sd = 0.f, sn = 0.f;
          const gu64* pr = pg + ((now - 1u) & 63u) * 256 + lane;
#pragma unroll
          for (int i = 0; i < 4; ++i) {
            const unsigned long long x = __hip_atomic_load(pr + i * 64, RLX_AGENT);
            const bool ok = (unsigned)(x >> 32) == tg4[i];
            sd += ok ? (float)(int)((unsigned)x - v) : 0.f; sn += ok ? 1.f : 0.f;
          }
          sd = wave_sum(sd); sn = wave_sum(sn);
          if (lane < 32 && sn >= 128.f) __hip_atomic_store(pm + lane * 64, ((unsigned long long)tag << 32) | (v + (unsigned)(int)(sd / sn)), RLX_AGENT);
        }
        const unsigned long long m = __hip_atomic_load(pm + (g & 31) * 64, RLX_AGENT);
        if (lane == 0 && (unsigned)(m >> 32) == tg4[0]) { __hip_atomic_store(lv + 4, (unsigned)m, RLX_WG); __hip_atomic_store(lv + 5, 1u, RLX_WG); }
        __builtin_amdgcn_s_sleep(MSLEEP);
      }
    }
  }
  wg_barrier();
  {
    float ov = 0.f;
#pragma unroll
    for (int i = 0; i < 4; ++i) ov += lds[L_RED + i * 128 + rh * 64 + lane];
    f32x2 yf = *reinterpret_cast<const f32x2*>(&lds[L_YF + cb * 128 + 8 * c + 2 * r]);
    Acc8 ay; acc_zero(ay);
    static_for<0, 16>([&](auto t) {
      const float o1 = dpp<BCAST + t.v>(ov);
      const f32x2 o2 = {o1, o1};
#pragma unroll
      for (int k = 0; k < 4; ++k) ay.a[k] = __builtin_elementwise_fma(o2, wf[t.v].a[k], ay.a[k]);
    });
    float y0, y1;
    xrow_reduce8(ay, y0, y1);
    if (rh == 1) { y0 += yf[0]; y1 += yf[1]; }
    lds[L_QP + w * 64 + lane] = rh ? y0 : y1;
    wg_barrier();
    const float yo = lds[L_QP + (w ^ 4) * 64 + lane];
    const float ym = rh ? y1 : y0;
    const float ys = rh ? (yo + ym) : (ym + yo);
    gu64* gp = (gu64*)(a.ws + WS_G) + ((size_t)(b * 64 + pidx)) * 512 + (cb * 2 + rh) * 64 + lane;
    __hip_atomic_store(gp, ((unsigned long long)tag << 32) | __float_as_uint(ys), RLX_AGENT);
  }
  STAMP(6);
  if (w != 0) { STAMP(7); STAMP_FLUSH(8); return; }
  {
    const gu64* gp = (const gu64*)(a.ws + WS_G) + (size_t)(b * 64 + (lane >> 3)) * 512 + pidx * 8 + (lane & 7);
    constexpr int NG = 8, GS = 8 * 512;
    const int P = pidx * 8 + (lane & 7), pl = P & 63;
    const int col = (P >> 7) * 128 + 8 * (pl & 15) + 2 * (pl >> 4) + ((P >> 6) & 1);
    const float xv = lo16(__builtin_nontemporal_load(reinterpret_cast<const unsigned short*>(a.x) + b * DM + col));
    unsigned val[NG];
    bool ok = false;
    for (unsigned spins = 0; spins < SPIN_CAP; ++spins) {
      if (!ok) {
        bool o = true;
#pragma unroll
        for (int i = 0; i < NG; ++i) { const unsigned long long v = __hip_atomic_load(gp + i * GS, RLX_AGENT); val[i] = (unsigned)v; o &= (unsigned)(v >> 32) == tag; }
        ok = o;
      }
      if (__all(ok)) break;
      __builtin_amdgcn_s_sleep(GSLEEP);
    }
    float t = 0.f;
#pragma unroll
    for (int i = 0; i < NG; ++i) t += __uint_as_float(val[i]);
    t += dpp<ROR8>(t);
    t = swap16_add(t, t);
    t = swap32_add(t, t);
    if (lane < 8) reinterpret_cast<__bf16*>(a.out)[b * DM + col] = (__bf16)(xv + t);
    if (pidx == 0 && lane == 0) __hip_atomic_store((gu32*)(a.ws + WS_EPOCH + b * 256), tag, RLX_AGENT);
  }
  STAMP(7);
  STAMP_FLUSH(8);
}

extern "C" void kernel_launch(void* const* d_in, const int* in_sizes, int n_in, void* d_out, int out_size, void* d_ws, size_t ws_size,
                              hipStream_t stream) {
  Args a;
  a.x = (const char*)d_in[0]; a.k = (const char*)d_in[1]; a.v = (const char*)d_in[2]; a.wq = (const char*)d_in[3]; a.wo = (const char*)d_in[4];
  a.wg = (const char*)d_in[6]; a.wu = (const char*)d_in[7]; a.wd = (const char*)d_in[8];
  a.out = (char*)d_out; a.ws = (char*)d_ws;
  hipLaunchKernelGGL(fused_decode_kernel, dim3(NWG), dim3(NTHR), 0, stream, a);
}
```

```cpp
#include <hip/hip_runtime.h>
#include <hip/hip_bf16.h>
#include <stdint.h>

typedef unsigned u32x4 __attribute__((ext_vector_type(4)));
typedef unsigned u32x2 __attribute__((ext_vector_type(2)));
typedef float f32x2 __attribute__((ext_vector_type(2)));
typedef float f32x4 __attribute__((ext_vector_type(4)));
typedef __attribute__((ext_vector_type(2))) __bf16 bf16x2_t;
#define GAS __attribute__((address_space(1)))
typedef GAS unsigned gu32;
typedef GAS unsigned long long gu64;
#define RLX_AGENT __ATOMIC_RELAXED, __HIP_MEMORY_SCOPE_AGENT
#define RLX_WG __ATOMIC_RELAXED, __HIP_MEMORY_SCOPE_WORKGROUP

constexpr int DM = 512, NH = 32, SK = 8192, DH = 128, FF = 1792;
constexpr int NWG = 256, NTHR = 512;
constexpr int D = 8, DMID = 6, DLO = 4;
constexpr int R = 16;
constexpr int A = 50;
constexpr int HD = 8;
constexpr int NIT = 16;
constexpr int NUN = ((2 * (A - D) + 15) / 16) * 16;
constexpr int TAUK = 228, TAUV = 222, TMARG = 30;
constexpr unsigned SPIN_CAP = 1u << 18;
static_assert(D < R && NUN <= 16 * (NIT - 1), "schedule");
constexpr int joff(int J) { return (J >> 2) * 16384 + (J & 3) * 1024; }
__device__ constexpr size_t itoff(int it) { return (size_t)it << 17; }

constexpr size_t WS_EPOCH = 0;
constexpr size_t WS_LSE = 4096;
constexpr size_t WS_MEAN = 32768;
constexpr size_t WS_PROG = 65536;
constexpr size_t WS_G = 262144;

constexpr int L_QP = 0;
constexpr int L_RED = 512;
constexpr int L_GU = 1024;
constexpr int L_YF = 1280;
constexpr int L_CNT = 1792;
constexpr int L_VT = 1800;
constexpr int L_TAG = 1808;
constexpr int L_WO = 2048;
constexpr int L_FLOATS = L_WO + 32768;

template <int OFF> __device__ __forceinline__ void ld16(u32x4& d, unsigned voff, const void* sb) {
  asm volatile("global_load_dwordx4 %0, %1, %2 offset:%3 nt" : "=v"(d) : "v"(voff), "s"(sb), "i"(OFF) : "memory");
}
template <int N, int OFF> __device__ __forceinline__ void pace_ld(u32x4& nxt, unsigned voff, const void* sb) {
  asm volatile("s_waitcnt vmcnt(%3)\n\tglobal_load_dwordx4 %0, %1, %2 offset:%4 nt" : "=v"(nxt) : "v"(voff), "s"(sb), "i"(N), "i"(OFF) : "memory");
}
template <int N, int OFF> __device__ __forceinline__ void wait_ld(u32x4& cur, u32x4& nxt, unsigned voff, const void* sb) {
  asm volatile("s_waitcnt vmcnt(%4)\n\tglobal_load_dwordx4 %1, %2, %3 offset:%5 nt" : "+v"(cur), "+v"(nxt) : "v"(voff), "s"(sb), "i"(N), "i"(OFF) : "memory");
}
template <int OFF> __device__ __forceinline__ void wait_ld_lvl(u32x4& cur, u32x4& nxt, unsigned voff, const void* sb, int lvl) {
  asm volatile("s_cmp_lt_i32 %4, 1\n\ts_cbranch_scc1 .Lthr%=\n\ts_waitcnt vmcnt(%6)\n\ts_cmp_lt_i32 %4, 2\n\ts_cbranch_scc1 .Lthr%=\n\ts_waitcnt vmcnt(%7)\n.Lthr%=:\n\t"
               "s_waitcnt vmcnt(%5)\n\tglobal_load_dwordx4 %1, %2, %3 offset:%8 nt"
               : "+v"(cur), "+v"(nxt) : "v"(voff), "s"(sb), "s"(lvl), "i"(D - 1), "i"(DMID - 1), "i"(DLO - 1), "i"(OFF) : "memory", "scc");
}
#define SBAR() __builtin_amdgcn_sched_barrier(0)
template <int N> __device__ __forceinline__ void wait_vm(u32x4& r) { asm volatile("s_waitcnt vmcnt(%1)" : "+v"(r) : "i"(N) : "memory"); }
__device__ __forceinline__ void landed(u32x4& r) { asm volatile("" : "+v"(r)); }
__device__ __forceinline__ void wg_barrier() { asm volatile("s_waitcnt lgkmcnt(0)\n\ts_barrier" ::: "memory"); }

template <int CTRL> __device__ __forceinline__ float dpp(float x) {
  return __builtin_bit_cast(float, __builtin_amdgcn_mov_dpp(__builtin_bit_cast(int, x), CTRL, 0xf, 0xf, true));
}
constexpr int XOR1 = 0xB1, XOR2 = 0x4E, HMIR = 0x141, RMIR = 0x140, ROR4 = 0x124, ROR8 = 0x128, BCAST = 0x150;
__device__ __forceinline__ float lo16(unsigned u) { return __builtin_bit_cast(float, u << 16); }
__device__ __forceinline__ float hi16(unsigned u) { return __builtin_bit_cast(float, u & 0xffff0000u); }
__device__ __forceinline__ float dot2(unsigned k, unsigned q, float acc) {
  return __builtin_amdgcn_fdot2_f32_bf16(__builtin_bit_cast(bf16x2_t, k), __builtin_bit_cast(bf16x2_t, q), acc, false);
}
__device__ __forceinline__ unsigned pack_bf16(float lo, float hi) {
  bf16x2_t v = {(__bf16)lo, (__bf16)hi};
  return __builtin_bit_cast(unsigned, v);
}
__device__ __forceinline__ float swap32_add(float a, float b) {
  auto s = __builtin_amdgcn_permlane32_swap(__float_as_uint(a), __float_as_uint(b), false, false);
  return __uint_as_float(s[0]) + __uint_as_float(s[1]);
}
__device__ __forceinline__ float swap16_add(float a, float b) {
  auto s = __builtin_amdgcn_permlane16_swap(__float_as_uint(a), __float_as_uint(b), false, false);
  return __uint_as_float(s[0]) + __uint_as_float(s[1]);
}
__device__ __forceinline__ float swap32_max(float a) {
  auto s = __builtin_amdgcn_permlane32_swap(__float_as_uint(a), __float_as_uint(a), false, false);
  return fmaxf(__uint_as_float(s[0]), __uint_as_float(s[1]));
}
__device__ __forceinline__ float swap16_max(float a) {
  auto s = __builtin_amdgcn_permlane16_swap(__float_as_uint(a), __float_as_uint(a), false, false);
  return fmaxf(__uint_as_float(s[0]), __uint_as_float(s[1]));
}
__device__ __forceinline__ float row_sum(float x) { x += dpp<XOR1>(x); x += dpp<XOR2>(x); x += dpp<HMIR>(x); x += dpp<RMIR>(x); return x; }
__device__ __forceinline__ float row_max(float x) {
  x = fmaxf(x, dpp<XOR1>(x)); x = fmaxf(x, dpp<XOR2>(x)); x = fmaxf(x, dpp<HMIR>(x)); x = fmaxf(x, dpp<RMIR>(x)); return x;
}
__device__ __forceinline__ float wave_sum(float x) { x = row_sum(x); x = swap16_add(x, x); return swap32_add(x, x); }
__device__ __forceinline__ float wave_max(float x) { x = row_max(x); x = swap16_max(x); return swap32_max(x); }
struct Acc8 { f32x2 a[4]; };
__device__ __forceinline__ void acc_zero(Acc8& acc) {
#pragma unroll
  for (int k = 0; k < 4; ++k) acc.a[k] = f32x2{0.f, 0.f};
}
__device__ __forceinline__ void pin(Acc8& acc) { asm volatile("" : "+v"(acc.a[0]), "+v"(acc.a[1]), "+v"(acc.a[2]), "+v"(acc.a[3])); }
__device__ __forceinline__ void fma8(Acc8& acc, float s, const u32x4& w) {
  const f32x2 s2 = {s, s};
#pragma unroll
  for (int k = 0; k < 4; ++k) acc.a[k] = __builtin_elementwise_fma(s2, f32x2{lo16(w[k]), hi16(w[k])}, acc.a[k]);
  pin(acc);
}
__device__ __forceinline__ void unpack8(Acc8& o, const u32x4& w) {
#pragma unroll
  for (int k = 0; k < 4; ++k) o.a[k] = f32x2{lo16(w[k]), hi16(w[k])};
  pin(o);
}
__device__ __forceinline__ void xrow_reduce8(const Acc8& x, float& u0, float& u1) {
  const float t0 = swap32_add(x.a[0][0], x.a[2][0]), t1 = swap32_add(x.a[0][1], x.a[2][1]), t2 = swap32_add(x.a[1][0], x.a[3][0]), t3 = swap32_add(x.a[1][1], x.a[3][1]);
  u0 = swap16_add(t0, t2); u1 = swap16_add(t1, t3);
}
template <int T> struct IC { static constexpr int v = T; };
template <int I, int N, class F> __device__ __forceinline__ void static_for(F&& f) { if constexpr (I < N) { f(IC<I>{}); static_for<I + 1, N>(f); } }
__device__ __forceinline__ void lds_wait_count(float* lds, int idx, unsigned want) {
  for (unsigned spins = 0; spins < (SPIN_CAP << 2); ++spins) {
    const unsigned cnt = __hip_atomic_load(reinterpret_cast<unsigned*>(lds) + idx, RLX_WG);
    if (__builtin_amdgcn_readfirstlane(cnt) >= want) break;
    __builtin_amdgcn_s_sleep(1);
  }
  asm volatile("" ::: "memory");
}
__device__ __forceinline__ void lds_arrive(float* lds, int idx, int lane) {
  asm volatile("s_waitcnt lgkmcnt(0)" ::: "memory");
  if (lane == 0) __hip_atomic_fetch_add(reinterpret_cast<unsigned*>(lds) + idx, 1u, RLX_WG);
}
constexpr int cu_issued(int n) { return n < 2 * (A - D) ? A + n / 2 + 1 : n + D + 1; }

struct Args {
  const char* x; const char* k; const char* v; const char* wq; const char* wo; const char* wg; const char* wu; const char* wd;
  char* out; char* ws;
};

__global__ void __launch_bounds__(NTHR, 2) fused_decode_kernel(Args a) {
  __shared__ __attribute__((aligned(16))) float lds[L_FLOATS];
  const int tid = threadIdx.x, lane = tid & 63, w = __builtin_amdgcn_readfirstlane(tid >> 6);
  const int g = blockIdx.x, xcd = g & 7;
  const int s = (g >> 3) & 1, hh = (g >> 4) & 3, b = g >> 6, h = hh * 8 + xcd;
  const int r = lane >> 4, c = lane & 15;
  const int pidx = h * 2 + s;
  const int cb = w & 3, rh = w >> 2;
  Acc8 wf[16];
  unsigned tag;

  if (w < 4) {
    auto level = [&](int sched) -> int {
      const unsigned V = (unsigned)__builtin_amdgcn_s_memrealtime() - (unsigned)sched;
      unsigned* lv = reinterpret_cast<unsigned*>(lds) + L_VT;
      __hip_atomic_store(lv + w, V, RLX_WG);
      const unsigned vb_ = __hip_atomic_load(lv + 4, RLX_WG), ok = __hip_atomic_load(lv + 5, RLX_WG);
      const int d = (int)(V - vb_);
      return __builtin_amdgcn_readfirstlane(ok == 0u ? 1 : (d < -TMARG ? 2 : (d < TMARG ? 1 : 0)));
    };
    const size_t kvoff = (size_t)(b * NH + h) * SK * (DH * 2) + ((size_t)s << 16) + w * 4096;
    const char* kb = a.k + kvoff;
    const char* vb = a.v + kvoff;
    const unsigned vo = lane * 16;
    u32x4 win[NUN];
    u32x4 rs[R];
#pragma unroll
    for (int i = 0; i < R; ++i) rs[i] = u32x4{0u, 0u, 0u, 0u};
    static_for<0, A>([&](auto i) {
      constexpr int I = i.v;
      if constexpr (I < DMID) ld16<(joff(I & 15) & 4095)>(win[I], vo, kb + itoff(I >> 4) + (joff(I & 15) & ~4095));
      else pace_ld<DMID - 1, (joff(I & 15) & 4095)>(win[I], vo, kb + itoff(I >> 4) + (joff(I & 15) & ~4095));
    });
    wg_barrier();
    unsigned qh[4], ql[4];
    {
      float q0 = 0.f, q1 = 0.f;
#pragma unroll
      for (int i = 0; i < 4; ++i) { const f32x2 t = *reinterpret_cast<const f32x2*>(&lds[L_QP + i * 128 + 8 * c + 2 * r]); q0 += t[0]; q1 += t[1]; }
      constexpr float QS = 0.08838834764831845f * 1.4426950408889634f;
      q0 *= QS; q1 *= QS;
      const unsigned ph = pack_bf16(q0, q1);
      const unsigned pl = pack_bf16(q0 - lo16(ph), q1 - hi16(ph));
      {
        auto s1 = __builtin_amdgcn_permlane16_swap(ph, ph, false, false);
        auto e = __builtin_amdgcn_permlane32_swap(s1[0], s1[0], false, false);
        auto o = __builtin_amdgcn_permlane32_swap(s1[1], s1[1], false, false);
        qh[0] = e[0]; qh[2] = e[1]; qh[1] = o[0]; qh[3] = o[1];
      }
      {
        auto s1 = __builtin_amdgcn_permlane16_swap(pl, pl, false, false);
        auto e = __builtin_amdgcn_permlane32_swap(s1[0], s1[0], false, false);
        auto o = __builtin_amdgcn_permlane32_swap(s1[1], s1[1], false, false);
        ql[0] = e[0]; ql[2] = e[1]; ql[1] = o[0]; ql[3] = o[1];
      }
    }
    float sc[NIT];
#pragma unroll
    for (int i = 0; i < NIT; ++i) sc[i] = 0.f;
    const bool b0 = (c & 1) != 0, b1 = (c & 2) != 0;
    const int cq = c >> 2;
    auto reduce4 = [&](const float (&p)[4]) -> float {
      const float k0 = b0 ? p[1] : p[0], s0 = b0 ? p[0] : p[1];
      const float k1 = b0 ? p[3] : p[2], s1 = b0 ? p[2] : p[3];
      const float a0 = k0 + dpp<XOR1>(s0), a1 = k1 + dpp<XOR1>(s1);
      const float k2 = b1 ? a1 : a0, s2 = b1 ? a0 : a1;
      float t = k2 + dpp<XOR2>(s2);
      t += dpp<ROR4>(t); t += dpp<ROR8>(t);
      return t;
    };
    auto kdot = [&](const u32x4& kv) -> float {
      float d0 = dot2(kv[0], qh[0], 0.f), d1 = dot2(kv[0], ql[0], 0.f);
#pragma unroll
      for (int k = 1; k < 4; ++k) { d0 = dot2(kv[k], qh[k], d0); d1 = dot2(kv[k], ql[k], d1); }
      return d0 + d1;
    };
    {
      float part[4], snew = 0.f;
      static_for<0, NUN>([&](auto j) {
        constexpr int N = j.v;
        constexpr int I = cu_issued(N) - 1;
        constexpr bool issues = (N == 0) || (cu_issued(N) != cu_issued(N - 1));
        if constexpr (issues) {
          if constexpr (I < NUN) pace_ld<DMID - 1, (joff(I & 15) & 4095)>(win[I], vo, kb + itoff(I >> 4) + (joff(I & 15) & ~4095));
          else wait_ld<DMID - 1, (joff(I & 15) & 4095)>(win[N], rs[I % R], vo, kb + itoff(I >> 4) + (joff(I & 15) & ~4095));
        }
        landed(win[N]);
        part[N & 3] = kdot(win[N]);
        if constexpr ((N & 3) == 3) { const float t = reduce4(part); snew = (cq == ((N & 15) >> 2)) ? t : snew; }
        if constexpr ((N & 15) == 15) sc[NIT - NUN / 16 + N / 16] = snew;
        SBAR();
      });
    }
    for (int it = NUN / 16; it < NIT; ++it) {
      const char* pc = kb + itoff(it);
      const char* pn = it < NIT - 1 ? kb + itoff(it + 1) : vb;
      float part[4], snew = 0.f;
      const int lvl = level(it * TAUK);
      static_for<0, 16>([&](auto j) {
        constexpr int J = j.v;
        wait_ld_lvl<(joff((J + D) & 15) & 4095)>(rs[J % R], rs[(J + D) % R], vo, ((J + D >= 16) ? pn : pc) + (joff((J + D) & 15) & ~4095), lvl);
        part[J & 3] = kdot(rs[J % R]);
        if constexpr ((J & 3) == 3) { const float t = reduce4(part); snew = (cq == (J >> 2)) ? t : snew; }
        SBAR();
      });
#pragma unroll
      for (int i = 0; i < NIT - 1; ++i) sc[i] = sc[i + 1];
      sc[NIT - 1] = snew;
    }
    lds_wait_count(lds, L_CNT, 4u);
    tag = __builtin_amdgcn_readfirstlane(reinterpret_cast<const unsigned*>(lds)[L_TAG + b]);
    float mw;
    {
      float m = sc[0];
#pragma unroll
      for (int i = 1; i < NIT; ++i) m = fmaxf(m, sc[i]);
      mw = wave_max(m);
      float l = 0.f;
#pragma unroll
      for (int i = 0; i < NIT; ++i) { sc[i] = __builtin_amdgcn_exp2f(sc[i] - mw); l += sc[i]; }
      l = wave_sum(l);
      const float lse = mw + __builtin_amdgcn_logf(l);
      if (lane == 0)
        __hip_atomic_store((gu64*)(a.ws + WS_LSE) + (b * NH + h) * 8 + s * 4 + w, ((unsigned long long)tag << 32) | __float_as_uint(lse), RLX_AGENT);
    }
    Acc8 acc; acc_zero(acc);
    float coef = 0.f;
    for (int it = 0; it < NIT - 2; ++it) {
      const char* pc = vb + itoff(it);
      const char* pn = vb + itoff(it + 1);
      const float pcur = sc[0];
      const int lvl = level(16 * TAUK + it * TAUV);
      static_for<0, 16>([&](auto j) {
        constexpr int J = j.v;
        wait_ld_lvl<(joff((J + D) & 15) & 4095)>(rs[J % R], rs[(J + D) % R], vo, ((J + D >= 16) ? pn : pc) + (joff((J + D) & 15) & ~4095), lvl);
        fma8(acc, dpp<BCAST + J>(pcur), rs[J % R]);
        SBAR();
      });
#pragma unroll
      for (int i = 0; i < NIT - 1; ++i) sc[i] = sc[i + 1];
    }
    {
      const char* pc = vb + itoff(NIT - 2);
      const char* pn = vb + itoff(NIT - 1);
      const float pcur = sc[0];
      lds_wait_count(lds, L_CNT + 1, 4u);
      if (lane == 0) __hip_atomic_fetch_add(reinterpret_cast<unsigned*>(lds) + L_VT + 6, 1u, RLX_WG);
      const int lvl = level(16 * TAUK + (NIT - 2) * TAUV);
      static_for<0, 16>([&](auto j) {
        constexpr int J = j.v;
        const u32x4 wt = *reinterpret_cast<const u32x4*>(&lds[L_WO + (r * 16 + J) * 256 + cb * 64 + c * 4]);
        wait_ld_lvl<(joff((J + D) & 15) & 4095)>(rs[J % R], rs[(J + D) % R], vo, ((J + D >= 16) ? pn : pc) + (joff((J + D) & 15) & ~4095), lvl);
        fma8(acc, dpp<BCAST + J>(pcur), rs[J % R]);
        unpack8(wf[J], wt);
        SBAR();
      });
    }
    {
      u32x2 lg;
      const gu64* lsep = (const gu64*)(a.ws + WS_LSE) + (b * NH + h) * 8 + (c & 7);
      asm volatile("global_load_dwordx2 %0, %1, off sc1" : "=v"(lg) : "v"(lsep) : "memory");
      const char* pc = vb + itoff(NIT - 1);
      const float pcur = sc[1];
      const int lvl = level(16 * TAUK + (NIT - 1) * TAUV);
      static_for<0, 16>([&](auto j) {
        constexpr int J = j.v;
        if constexpr (J + D < 16) wait_ld_lvl<(joff((J + D) & 15) & 4095)>(rs[J % R], rs[(J + D) % R], vo, pc + (joff((J + D) & 15) & ~4095), lvl);
        else wait_vm<15 - J>(rs[J % R]);
        if constexpr (J == D + 1) {
          asm volatile("" : "+v"(lg));
          unsigned lv = lg[0], lt = lg[1];
          for (unsigned spins = 0; spins < SPIN_CAP; ++spins) {
            if (__all(lt == tag)) break;
            __builtin_amdgcn_s_sleep(8);
            const unsigned long long v = __hip_atomic_load(lsep, RLX_AGENT);
            lv = (unsigned)v; lt = (unsigned)(v >> 32);
          }
          const float le = __uint_as_float(lv);
          const float M = row_max(le);
          const float L = 0.5f * row_sum(__builtin_amdgcn_exp2f(le - M));
          coef = __builtin_amdgcn_exp2f(mw - M) / L;
        }
        fma8(acc, dpp<BCAST + J>(pcur), rs[J % R]);
        SBAR();
      });
    }
    float u0, u1;
    xrow_reduce8(acc, u0, u1);
    *reinterpret_cast<f32x2*>(&lds[L_RED + w * 128 + 8 * c + 2 * r]) = f32x2{u0 * coef, u1 * coef};
  } else {
    const int hw = w - 4;
    const int cg = hh * 2 + s;
    const int f0 = xcd * 224 + (cg < 7 ? cg : 6) * 32;
    u32x4 xc; unsigned xo0, xo1;
    {
      const char* xb = a.x + b * (DM * 2);
      ld16<0>(xc, lane * 16, xb);
      asm volatile("global_load_ushort %0, %1, %2 nt" : "=v"(xo0) : "v"((unsigned)(lane * 2)), "s"(xb + hw * 256) : "memory");
      asm volatile("global_load_ushort %0, %1, %2 offset:128 nt" : "=v"(xo1) : "v"((unsigned)(lane * 2)), "s"(xb + hw * 256) : "memory");
    }
    const char* wqb = a.wq + (size_t)(hw * 128) * (NH * DH * 2) + h * (DH * 2);
    const unsigned voq = (unsigned)(r * 16) * (NH * DH * 2) + c * 16;
    u32x4 wt[32];
    static_for<0, HD>([&](auto t) { ld16<0>(wt[t.v], voq, wqb + ((t.v >> 4) * 64 + (t.v & 15)) * (NH * DH * 2)); });
    if (tid == 256) { lds[L_CNT] = 0.f; lds[L_CNT + 1] = 0.f; lds[L_VT + 5] = 0.f; lds[L_VT + 6] = 0.f; }
    asm volatile("s_waitcnt vmcnt(%3)" : "+v"(xc), "+v"(xo0), "+v"(xo1) : "i"(HD) : "memory");
    float xn[2];
    {
      float xv[8];
#pragma unroll
      for (int k = 0; k < 4; ++k) { xv[2 * k] = lo16(xc[k]); xv[2 * k + 1] = hi16(xc[k]); }
      float sm = ((xv[0] + xv[1]) + (xv[2] + xv[3])) + ((xv[4] + xv[5]) + (xv[6] + xv[7]));
      const float mean = wave_sum(sm) * (1.0f / DM);
      float sq = 0.f;
#pragma unroll
      for (int k = 0; k < 8; ++k) { const float d = xv[k] - mean; sq = fmaf(d, d, sq); }
      const float var = wave_sum(sq) * (1.0f / DM);
      const float rstd = 1.0f / sqrtf(var + 1e-5f);
      xn[0] = (lo16(xo0) - mean) * rstd;
      xn[1] = (lo16(xo1) - mean) * rstd;
    }
    {
      Acc8 acc; acc_zero(acc);
      SBAR();
      static_for<0, 32>([&](auto t) {
        constexpr int T = t.v, I = T + HD;
        wait_vm<(I <= 32 ? HD - 1 : 31 - T)>(wt[T]);
        if constexpr (I < 32) ld16<0>(wt[I], voq, wqb + ((I >> 4) * 64 + (I & 15)) * (NH * DH * 2));
        fma8(acc, dpp<BCAST + (T & 15)>(xn[T >> 4]), wt[T]);
        SBAR();
      });
      float u0, u1;
      xrow_reduce8(acc, u0, u1);
      *reinterpret_cast<f32x2*>(&lds[L_QP + hw * 128 + 8 * c + 2 * r]) = f32x2{u0, u1};
    }
    wg_barrier();
    if (hw == 0 && lane < 4) reinterpret_cast<unsigned*>(lds)[L_TAG + lane] = __hip_atomic_load((const gu32*)(a.ws + WS_EPOCH + lane * 256), RLX_AGENT) + 1u;
    {
      Acc8 ag, au; acc_zero(ag); acc_zero(au);
      const unsigned vog = (unsigned)((lane >> 2) * 4) * (FF * 2) + (lane & 3) * 16;
      const size_t sog = (size_t)(hw * 128) * (FF * 2) + f0 * 2;
      static_for<0, 8>([&](auto t) {
        constexpr int T = t.v;
        u32x4 gt, ut;
        ld16<0>(gt, vog, a.wg + sog + ((T >> 2) * 64 + (T & 3)) * (FF * 2));
        ld16<0>(ut, vog, a.wu + sog + ((T >> 2) * 64 + (T & 3)) * (FF * 2));
        const float xb_ = dpp<(T & 3) * 0x55>(xn[T >> 2]);
        wait_vm<1>(gt); fma8(ag, xb_, gt);
        wait_vm<0>(ut); fma8(au, xb_, ut);
        SBAR();
      });
      float t8[8];
#pragma unroll
      for (int k = 0; k < 8; ++k) t8[k] = swap32_add(ag.a[k >> 1][k & 1], au.a[k >> 1][k & 1]);
#pragma unroll
      for (int k = 0; k < 4; ++k) {
        float t = swap16_add(t8[k], t8[k + 4]); t += dpp<ROR4>(t); t += dpp<ROR8>(t);
        if ((lane & 12) == 0) lds[L_GU + ((r >> 1) * 32 + 8 * (lane & 3) + 4 * (r & 1) + k) * 4 + hw] = t;
      }
      lds_arrive(lds, L_CNT, lane);
      u32x4 wd[8];
      const char* wdb = a.wd + (size_t)f0 * (DM * 2) + hw * 256;
      const unsigned vod = (unsigned)(r * 8) * (DM * 2) + c * 16;
      static_for<0, 8>([&](auto t) {
        constexpr int T = t.v;
        if constexpr (T < 2) ld16<(T & 3) * 1024>(wd[T], vod, wdb + (T >> 2) * 4096);
        else pace_ld<1, (T & 3) * 1024>(wd[T], vod, wdb + (T >> 2) * 4096);
      });
      lds_wait_count(lds, L_CNT, 4u);
      const int f = r * 8 + (c & 7);
      const f32x4 g4 = *reinterpret_cast<const f32x4*>(&lds[L_GU + f * 4]), u4 = *reinterpret_cast<const f32x4*>(&lds[L_GU + (32 + f) * 4]);
      const float gs = (g4[0] + g4[1]) + (g4[2] + g4[3]), us = (u4[0] + u4[1]) + (u4[2] + u4[3]);
      const float hv = gs / (1.0f + __expf(-gs)) * us;
      Acc8 ay; acc_zero(ay);
      asm volatile("s_waitcnt vmcnt(0)" : "+v"(wd[0]), "+v"(wd[1]), "+v"(wd[2]), "+v"(wd[3]), "+v"(wd[4]), "+v"(wd[5]), "+v"(wd[6]), "+v"(wd[7]) :: "memory");
      if (cg < 7) static_for<0, 8>([&](auto t) { fma8(ay, dpp<BCAST + t.v>(hv), wd[t.v]); });
      float y0, y1;
      xrow_reduce8(ay, y0, y1);
      *reinterpret_cast<f32x2*>(&lds[L_YF + hw * 128 + 8 * c + 2 * r]) = f32x2{y0, y1};
    }
    {
      const char* wo0 = a.wo + (size_t)(h * DH + hw * 32) * (DM * 2);
      for (int i = 0; i < 32; i += 2) {
        u32x4 t0, t1;
        ld16<0>(t0, lane * 16, wo0 + i * 1024);
        ld16<1024>(t1, lane * 16, wo0 + i * 1024);
        wait_vm<1>(t0);
        *reinterpret_cast<u32x4*>(&lds[L_WO + (hw * 32 + i) * 256 + lane * 4]) = t0;
        wait_vm<0>(t1);
        *reinterpret_cast<u32x4*>(&lds[L_WO + (hw * 32 + i + 1) * 256 + lane * 4]) = t1;
      }
      lds_arrive(lds, L_CNT + 1, lane);
      lds_wait_count(lds, L_CNT + 1, 4u);
      static_for<0, 16>([&](auto t) { unpack8(wf[t.v], *reinterpret_cast<const u32x4*>(&lds[L_WO + (64 + r * 16 + t.v) * 256 + cb * 64 + c * 4])); });
    }
    tag = __builtin_amdgcn_readfirstlane(reinterpret_cast<const unsigned*>(lds)[L_TAG + b]);
    if (hw == 0) {
      unsigned* lv = reinterpret_cast<unsigned*>(lds) + L_VT;
      unsigned tg4[4];
#pragma unroll
      for (int i = 0; i < 4; ++i) tg4[i] = reinterpret_cast<const unsigned*>(lds)[L_TAG + i];
      gu64* pg = (gu64*)(a.ws + WS_PROG);
      gu64* pm = (gu64*)(a.ws + WS_MEAN);
      for (unsigned spins = 0; spins < (1u << 16); ++spins) {
        if (__builtin_amdgcn_readfirstlane(__hip_atomic_load(lv + 6, RLX_WG)) != 0u) break;
        const unsigned now = (unsigned)__builtin_amdgcn_s_memrealtime() >> 8;
        const unsigned v0 = __hip_atomic_load(lv, RLX_WG), v1 = __hip_atomic_load(lv + 1, RLX_WG), v2 = __hip_atomic_load(lv + 2, RLX_WG), v3 = __hip_atomic_load(lv + 3, RLX_WG);
        const unsigned v = v0 + (unsigned)(((int)(v1 - v0) + (int)(v2 - v0) + (int)(v3 - v0)) >> 2);
        if (lane == 0) __hip_atomic_store(pg + (now & 63u) * 256 + g, ((unsigned long long)tag << 32) | v, RLX_AGENT);
        if (g == 0) {
          float sd = 0.f, sn = 0.f;
          const gu64* pr = pg + ((now - 1u) & 63u) * 256 + lane;
#pragma unroll
          for (int i = 0; i < 4; ++i) {
            const unsigned long long x = __hip_atomic_load(pr + i * 64, RLX_AGENT);
            const bool ok = (unsigned)(x >> 32) == tg4[i];
            sd += ok ? (float)(int)((unsigned)x - v) : 0.f; sn += ok ? 1.f : 0.f;
          }
          sd = wave_sum(sd); sn = wave_sum(sn);
          if (lane < 32 && sn >= 128.f) __hip_atomic_store(pm + lane * 64, ((unsigned long long)tag << 32) | (v + (unsigned)(int)(sd / sn)), RLX_AGENT);
        }
        const unsigned long long m = __hip_atomic_load(pm + (g & 31) * 64, RLX_AGENT);
        if (lane == 0 && (unsigned)(m >> 32) == tg4[0]) { __hip_atomic_store(lv + 4, (unsigned)m, RLX_WG); __hip_atomic_store(lv + 5, 1u, RLX_WG); }
        __builtin_amdgcn_s_sleep(48);
      }
    }
  }
  wg_barrier();
  {
    float ov = 0.f;
#pragma unroll
    for (int i = 0; i < 4; ++i) ov += lds[L_RED + i * 128 + rh * 64 + lane];
    const f32x2 yf = *reinterpret_cast<const f32x2*>(&lds[L_YF + cb * 128 + 8 * c + 2 * r]);
    Acc8 ay; acc_zero(ay);
    static_for<0, 16>([&](auto t) {
      const float o1 = dpp<BCAST + t.v>(ov);
      const f32x2 o2 = {o1, o1};
#pragma unroll
      for (int k = 0; k < 4; ++k) ay.a[k] = __builtin_elementwise_fma(o2, wf[t.v].a[k], ay.a[k]);
    });
    float y0, y1;
    xrow_reduce8(ay, y0, y1);
    if (rh == 1) { y0 += yf[0]; y1 += yf[1]; }
    lds[L_QP + w * 64 + lane] = rh ? y0 : y1;
    wg_barrier();
    const float yo = lds[L_QP + (w ^ 4) * 64 + lane];
    const float ys = (rh ? y1 : y0) + yo;
    gu64* gp = (gu64*)(a.ws + WS_G) + ((size_t)(b * 64 + pidx)) * 512 + (cb * 2 + rh) * 64 + lane;
    __hip_atomic_store(gp, ((unsigned long long)tag << 32) | __float_as_uint(ys), RLX_AGENT);
  }
  if (w != 0) return;
  {
    const int P = pidx * 8 + (lane & 7), pl = P & 63;
    const int col = (P >> 7) * 128 + 8 * (pl & 15) + 2 * (pl >> 4) + ((P >> 6) & 1);
    const gu64* gp = (const gu64*)(a.ws + WS_G) + (size_t)(b * 64 + (lane >> 3)) * 512 + P;
    const float xv = lo16(__builtin_nontemporal_load(reinterpret_cast<const unsigned short*>(a.x) + b * DM + col));
    unsigned val[8];
    bool ok = false;
    __builtin_amdgcn_s_sleep(24);
    for (unsigned spins = 0; spins < SPIN_CAP; ++spins) {
      if (!ok) {
        bool o = true;
#pragma unroll
        for (int i = 0; i < 8; ++i) { const unsigned long long v = __hip_atomic_load(gp + i * 4096, RLX_AGENT); val[i] = (unsigned)v; o &= (unsigned)(v >> 32) == tag; }
        ok = o;
      }
      if (__all(ok)) break;
      __builtin_amdgcn_s_sleep(4);
    }
    float t = 0.f;
#pragma unroll
    for (int i = 0; i < 8; ++i) t += __uint_as_float(val[i]);
    t += dpp<ROR8>(t);
    t = swap16_add(t, t);
    t = swap32_add(t, t);
    if (lane < 8) reinterpret_cast<__bf16*>(a.out)[b * DM + col] = (__bf16)(xv + t);
    if (pidx == 0 && lane == 0) __hip_atomic_store((gu32*)(a.ws + WS_EPOCH + b * 256), tag, RLX_AGENT);
  }
}

extern "C" void kernel_launch(void* const* d_in, const int* in_sizes, int n_in, void* d_out, int out_size, void* d_ws, size_t ws_size,
                              hipStream_t stream) {
  Args a;
  a.x = (const char*)d_in[0]; a.k = (const char*)d_in[1]; a.v = (const char*)d_in[2]; a.wq = (const char*)d_in[3]; a.wo = (const char*)d_in[4];
  a.wg = (const char*)d_in[6]; a.wu = (const char*)d_in[7]; a.wd = (const char*)d_in[8];
  a.out = (char*)d_out; a.ws = (char*)d_ws;
  hipLaunchKernelGGL(fused_decode_kernel, dim3(NWG), dim3(NTHR), 0, stream, a);
}
```

```cpp
#include <hip/hip_runtime.h>
#include <hip/hip_bf16.h>
#include <stdint.h>

typedef unsigned u32x4 __attribute__((ext_vector_type(4)));
typedef unsigned u32x2 __attribute__((ext_vector_type(2)));
typedef float f32x2 __attribute__((ext_vector_type(2)));
typedef float f32x4 __attribute__((ext_vector_type(4)));
typedef __attribute__((ext_vector_type(2))) __bf16 bf16x2_t;
#define GAS __attribute__((address_space(1)))
typedef GAS unsigned gu32;
typedef GAS unsigned long long gu64;
#define RLX_AGENT __ATOMIC_RELAXED, __HIP_MEMORY_SCOPE_AGENT
#define RLX_WG __ATOMIC_RELAXED, __HIP_MEMORY_SCOPE_WORKGROUP

constexpr int DM = 512, NH = 32, SK = 8192, DH = 128, FF = 1792;
constexpr int NWG = 256, NTHR = 512;
constexpr int D = 8, DMID = 6, DLO = 4;
constexpr int R = 16;
constexpr int A = 50;
constexpr int HD = 8;
constexpr int NIT = 16;
constexpr int NUN = ((2 * (A - D) + 15) / 16) * 16;
constexpr int TAUK = 228, TAUV = 222, TMARG = 30;
constexpr unsigned SPIN_CAP = 1u << 18;
static_assert(D < R && NUN <= 16 * (NIT - 1), "schedule");
constexpr int joff(int J) { return (J >> 2) * 16384 + (J & 3) * 1024; }
__device__ constexpr size_t itoff(int it) { return (size_t)it << 17; }

constexpr size_t WS_EPOCH = 0;
constexpr size_t WS_LSE = 4096;
constexpr size_t WS_MEAN = 32768;
constexpr size_t WS_PROG = 65536;
constexpr size_t WS_G = 262144;

constexpr int L_QP = 0;
constexpr int L_RED = 512;
constexpr int L_GU = 1024;
constexpr int L_YF = 1280;
constexpr int L_CNT = 1792;
constexpr int L_VT = 1800;
constexpr int L_TAG = 1808;
constexpr int L_WO = 2048;
constexpr int L_FLOATS = L_WO + 32768;

template <int OFF> __device__ __forceinline__ void ld16(u32x4& d, unsigned voff, const void* sb) {
  asm volatile("global_load_dwordx4 %0, %1, %2 offset:%3 nt" : "=v"(d) : "v"(voff), "s"(sb), "i"(OFF) : "memory");
}
template <int N, int OFF> __device__ __forceinline__ void pace_ld(u32x4& nxt, unsigned voff, const void* sb) {
  asm volatile("s_waitcnt vmcnt(%3)\n\tglobal_load_dwordx4 %0, %1, %2 offset:%4 nt" : "=v"(nxt) : "v"(voff), "s"(sb), "i"(N), "i"(OFF) : "memory");
}
template <int N, int OFF> __device__ __forceinline__ void wait_ld(u32x4& cur, u32x4& nxt, unsigned voff, const void* sb) {
  asm volatile("s_waitcnt vmcnt(%4)\n\tglobal_load_dwordx4 %1, %2, %3 offset:%5 nt" : "+v"(cur), "+v"(nxt) : "v"(voff), "s"(sb), "i"(N), "i"(OFF) : "memory");
}
template <int OFF> __device__ __forceinline__ void wait_ld_lvl(u32x4& cur, u32x4& nxt, unsigned voff, const void* sb, int lvl) {
  asm volatile("s_cmp_lt_i32 %4, 1\n\ts_cbranch_scc1 .Lthr%=\n\ts_waitcnt vmcnt(%6)\n\ts_cmp_lt_i32 %4, 2\n\ts_cbranch_scc1 .Lthr%=\n\ts_waitcnt vmcnt(%7)\n.Lthr%=:\n\t"
               "s_waitcnt vmcnt(%5)\n\tglobal_load_dwordx4 %1, %2, %3 offset:%8 nt"
               : "+v"(cur), "+v"(nxt) : "v"(voff), "s"(sb), "s"(lvl), "i"(D - 1), "i"(DMID - 1), "i"(DLO - 1), "i"(OFF) : "memory", "scc");
}
#define SBAR() __builtin_amdgcn_sched_barrier(0)
template <int N> __device__ __forceinline__ void wait_vm(u32x4& r) { asm volatile("s_waitcnt vmcnt(%1)" : "+v"(r) : "i"(N) : "memory"); }
__device__ __forceinline__ void landed(u32x4& r) { asm volatile("" : "+v"(r)); }
__device__ __forceinline__ void wg_barrier() { asm volatile("s_waitcnt lgkmcnt(0)\n\ts_barrier" ::: "memory"); }

template <int CTRL> __device__ __forceinline__ float dpp(float x) {
  return __builtin_bit_cast(float, __builtin_amdgcn_mov_dpp(__builtin_bit_cast(int, x), CTRL, 0xf, 0xf, true));
}
constexpr int XOR1 = 0xB1, XOR2 = 0x4E, HMIR = 0x141, RMIR = 0x140, ROR4 = 0x124, ROR8 = 0x128, BCAST = 0x150;
__device__ __forceinline__ float lo16(unsigned u) { return __builtin_bit_cast(float, u << 16); }
__device__ __forceinline__ float hi16(unsigned u) { return __builtin_bit_cast(float, u & 0xffff0000u); }
__device__ __forceinline__ float dot2(unsigned k, unsigned q, float acc) {
  return __builtin_amdgcn_fdot2_f32_bf16(__builtin_bit_cast(bf16x2_t, k), __builtin_bit_cast(bf16x2_t, q), acc, false);
}
__device__ __forceinline__ unsigned pack_bf16(float lo, float hi) {
  bf16x2_t v = {(__bf16)lo, (__bf16)hi};
  return __builtin_bit_cast(unsigned, v);
}
__device__ __forceinline__ float swap32_add(float a, float b) {
  auto s = __builtin_amdgcn_permlane32_swap(__float_as_uint(a), __float_as_uint(b), false, false);
  return __uint_as_float(s[0]) + __uint_as_float(s[1]);
}
__device__ __forceinline__ float swap16_add(float a, float b) {
  auto s = __builtin_amdgcn_permlane16_swap(__float_as_uint(a), __float_as_uint(b), false, false);
  return __uint_as_float(s[0]) + __uint_as_float(s[1]);
}
__device__ __forceinline__ float swap32_max(float a) {
  auto s = __builtin_amdgcn_permlane32_swap(__float_as_uint(a), __float_as_uint(a), false, false);
  return fmaxf(__uint_as_float(s[0]), __uint_as_float(s[1]));
}
__device__ __forceinline__ float swap16_max(float a) {
  auto s = __builtin_amdgcn_permlane16_swap(__float_as_uint(a), __float_as_uint(a), false, false);
  return fmaxf(__uint_as_float(s[0]), __uint_as_float(s[1]));
}
__device__ __forceinline__ float row_sum(float x) { x += dpp<XOR1>(x); x += dpp<XOR2>(x); x += dpp<HMIR>(x); x += dpp<RMIR>(x); return x; }
__device__ __forceinline__ float row_max(float x) {
  x = fmaxf(x, dpp<XOR1>(x)); x = fmaxf(x, dpp<XOR2>(x)); x = fmaxf(x, dpp<HMIR>(x)); x = fmaxf(x, dpp<RMIR>(x)); return x;
}
__device__ __forceinline__ float wave_sum(float x) { x = row_sum(x); x = swap16_add(x, x); return swap32_add(x, x); }
__device__ __forceinline__ float wave_max(float x) { x = row_max(x); x = swap16_max(x); return swap32_max(x); }
struct Acc8 { f32x2 a[4]; };
__device__ __forceinline__ void acc_zero(Acc8& acc) {
#pragma unroll
  for (int k = 0; k < 4; ++k) acc.a[k] = f32x2{0.f, 0.f};
}
__device__ __forceinline__ void pin(Acc8& acc) { asm volatile("" : "+v"(acc.a[0]), "+v"(acc.a[1]), "+v"(acc.a[2]), "+v"(acc.a[3])); }
__device__ __forceinline__ void fma8(Acc8& acc, float s, const u32x4& w) {
  const f32x2 s2 = {s, s};
#pragma unroll
  for (int k = 0; k < 4; ++k) acc.a[k] = __builtin_elementwise_fma(s2, f32x2{lo16(w[k]), hi16(w[k])}, acc.a[k]);
  pin(acc);
}
__device__ __forceinline__ void unpack8(Acc8& o, const u32x4& w) {
#pragma unroll
  for (int k = 0; k < 4; ++k) o.a[k] = f32x2{lo16(w[k]), hi16(w[k])};
  pin(o);
}
__device__ __forceinline__ void xrow_reduce8(const Acc8& x, float& u0, float& u1) {
  const float t0 = swap32_add(x.a[0][0], x.a[2][0]), t1 = swap32_add(x.a[0][1], x.a[2][1]), t2 = swap32_add(x.a[1][0], x.a[3][0]), t3 = swap32_add(x.a[1][1], x.a[3][1]);
  u0 = swap16_add(t0, t2); u1 = swap16_add(t1, t3);
}
template <int T> struct IC { static constexpr int v = T; };
template <int I, int N, class F> __device__ __forceinline__ void static_for(F&& f) { if constexpr (I < N) { f(IC<I>{}); static_for<I + 1, N>(f); } }
__device__ __forceinline__ void lds_wait_count(float* lds, int idx, unsigned want) {
  for (unsigned spins = 0; spins < (SPIN_CAP << 2); ++spins) {
    const unsigned cnt = __hip_atomic_load(reinterpret_cast<unsigned*>(lds) + idx, RLX_WG);
    if (__builtin_amdgcn_readfirstlane(cnt) >= want) break;
    __builtin_amdgcn_s_sleep(1);
  }
  asm volatile("" ::: "memory");
}
__device__ __forceinline__ void lds_arrive(float* lds, int idx, int lane) {
  asm volatile("s_waitcnt lgkmcnt(0)" ::: "memory");
  if (lane == 0) __hip_atomic_fetch_add(reinterpret_cast<unsigned*>(lds) + idx, 1u, RLX_WG);
}
constexpr int cu_issued(int n) { return n < 2 * (A - D) ? A + n / 2 + 1 : n + D + 1; }

struct Args {
  const char* x; const char* k; const char* v; const char* wq; const char* wo; const char* wg; const char* wu; const char* wd;
  char* out; char* ws;
};

__global__ void __launch_bounds__(NTHR, 2) fused_decode_kernel(Args a) {
  __shared__ __attribute__((aligned(16))) float lds[L_FLOATS];
  const int tid = threadIdx.x, lane = tid & 63, w = __builtin_amdgcn_readfirstlane(tid >> 6);
  const int g = blockIdx.x, xcd = g & 7;
  const int s = (g >> 3) & 1, hh = (g >> 4) & 3, b = g >> 6, h = hh * 8 + xcd;
  const int r = lane >> 4, c = lane & 15;
  const int pidx = h * 2 + s;
  const int cb = w & 3, rh = w >> 2;
  Acc8 wf[16];
  unsigned tag;

  if (w < 4) {
    auto level = [&](int sched) -> int {
      const unsigned V = (unsigned)__builtin_amdgcn_s_memrealtime() - (unsigned)sched;
      unsigned* lv = reinterpret_cast<unsigned*>(lds) + L_VT;
      __hip_atomic_store(lv + w, V, RLX_WG);
      const unsigned vb_ = __hip_atomic_load(lv + 4, RLX_WG), ok = __hip_atomic_load(lv + 5, RLX_WG);
      const int d = (int)(V - vb_);
      return __builtin_amdgcn_readfirstlane(ok == 0u ? 1 : (d < -TMARG ? 2 : (d < TMARG ? 1 : 0)));
    };
    const size_t kvoff = (size_t)(b * NH + h) * SK * (DH * 2) + ((size_t)s << 16) + w * 4096;
    const char* kb = a.k + kvoff;
    const char* vb = a.v + kvoff;
    const unsigned vo = lane * 16;
    u32x4 win[NUN];
    u32x4 rs[R];
#pragma unroll
    for (int i = 0; i < R; ++i) rs[i] = u32x4{0u, 0u, 0u, 0u};
    static_for<0, A>([&](auto i) {
      constexpr int I = i.v;
      if constexpr (I < DMID) ld16<(joff(I & 15) & 4095)>(win[I], vo, kb + itoff(I >> 4) + (joff(I & 15) & ~4095));
      else pace_ld<DMID - 1, (joff(I & 15) & 4095)>(win[I], vo, kb + itoff(I >> 4) + (joff(I & 15) & ~4095));
    });
    wg_barrier();
    unsigned qh[4], ql[4];
    {
      float q0 = 0.f, q1 = 0.f;
#pragma unroll
      for (int i = 0; i < 4; ++i) { const f32x2 t = *reinterpret_cast<const f32x2*>(&lds[L_QP + i * 128 + 8 * c + 2 * r]); q0 += t[0]; q1 += t[1]; }
      constexpr float QS = 0.08838834764831845f * 1.4426950408889634f;
      q0 *= QS; q1 *= QS;
      const unsigned ph = pack_bf16(q0, q1);
      const unsigned pl = pack_bf16(q0 - lo16(ph), q1 - hi16(ph));
      {
        auto s1 = __builtin_amdgcn_permlane16_swap(ph, ph, false, false);
        auto e = __builtin_amdgcn_permlane32_swap(s1[0], s1[0], false, false);
        auto o = __builtin_amdgcn_permlane32_swap(s1[1], s1[1], false, false);
        qh[0] = e[0]; qh[2] = e[1]; qh[1] = o[0]; qh[3] = o[1];
      }
      {
        auto s1 = __builtin_amdgcn_permlane16_swap(pl, pl, false, false);
        auto e = __builtin_amdgcn_permlane32_swap(s1[0], s1[0], false, false);
        auto o = __builtin_amdgcn_permlane32_swap(s1[1], s1[1], false, false);
        ql[0] = e[0]; ql[2] = e[1]; ql[1] = o[0]; ql[3] = o[1];
      }
    }
    float sc[NIT];
#pragma unroll
    for (int i = 0; i < NIT; ++i) sc[i] = 0.f;
    const bool b0 = (c & 1) != 0, b1 = (c & 2) != 0;
    const int cq = c >> 2;
    auto reduce4 = [&](const float (&p)[4]) -> float {
      const float k0 = b0 ? p[1] : p[0], s0 = b0 ? p[0] : p[1];
      const float k1 = b0 ? p[3] : p[2], s1 = b0 ? p[2] : p[3];
      const float a0 = k0 + dpp<XOR1>(s0), a1 = k1 + dpp<XOR1>(s1);
      const float k2 = b1 ? a1 : a0, s2 = b1 ? a0 : a1;
      float t = k2 + dpp<XOR2>(s2);
      t += dpp<ROR4>(t); t += dpp<ROR8>(t);
      return t;
    };
    auto kdot = [&](const u32x4& kv) -> float {
      float d0 = dot2(kv[0], qh[0], 0.f), d1 = dot2(kv[0], ql[0], 0.f);
#pragma unroll
      for (int k = 1; k < 4; ++k) { d0 = dot2(kv[k], qh[k], d0); d1 = dot2(kv[k], ql[k], d1); }
      return d0 + d1;
    };
    {
      float part[4], snew = 0.f;
      static_for<0, NUN>([&](auto j) {
        constexpr int N = j.v;
        constexpr int I = cu_issued(N) - 1;
        constexpr bool issues = (N == 0) || (cu_issued(N) != cu_issued(N - 1));
        if constexpr (issues) {
          if constexpr (I < NUN) pace_ld<DMID - 1, (joff(I & 15) & 4095)>(win[I], vo, kb + itoff(I >> 4) + (joff(I & 15) & ~4095));
          else wait_ld<DMID - 1, (joff(I & 15) & 4095)>(win[N], rs[I % R], vo, kb + itoff(I >> 4) + (joff(I & 15) & ~4095));
        }
        landed(win[N]);
        part[N & 3] = kdot(win[N]);
        if constexpr ((N & 3) == 3) { const float t = reduce4(part); snew = (cq == ((N & 15) >> 2)) ? t : snew; }
        if constexpr ((N & 15) == 15) sc[NIT - NUN / 16 + N / 16] = snew;
        SBAR();
      });
    }
    for (int it = NUN / 16; it < NIT; ++it) {
      const char* pc = kb + itoff(it);
      const char* pn = it < NIT - 1 ? kb + itoff(it + 1) : vb;
      float part[4], snew = 0.f;
      const int lvl = level(it * TAUK);
      static_for<0, 16>([&](auto j) {
        constexpr int J = j.v;
        wait_ld_lvl<(joff((J + D) & 15) & 4095)>(rs[J % R], rs[(J + D) % R], vo, ((J + D >= 16) ? pn : pc) + (joff((J + D) & 15) & ~4095), lvl);
        part[J & 3] = kdot(rs[J % R]);
        if constexpr ((J & 3) == 3) { const float t = reduce4(part); snew = (cq == (J >> 2)) ? t : snew; }
        SBAR();
      });
#pragma unroll
      for (int i = 0; i < NIT - 1; ++i) sc[i] = sc[i + 1];
      sc[NIT - 1] = snew;
    }
    lds_wait_count(lds, L_CNT, 4u);
    tag = __builtin_amdgcn_readfirstlane(reinterpret_cast<const unsigned*>(lds)[L_TAG + b]);
    float mw;
    {
      float m = sc[0];
#pragma unroll
      for (int i = 1; i < NIT; ++i) m = fmaxf(m, sc[i]);
      mw = wave_max(m);
      float l = 0.f;
#pragma unroll
      for (int i = 0; i < NIT; ++i) { sc[i] = __builtin_amdgcn_exp2f(sc[i] - mw); l += sc[i]; }
      l = wave_sum(l);
      const float lse = mw + __builtin_amdgcn_logf(l);
      if (lane == 0)
        __hip_atomic_store((gu64*)(a.ws + WS_LSE) + (b * NH + h) * 8 + s * 4 + w, ((unsigned long long)tag << 32) | __float_as_uint(lse), RLX_AGENT);
    }
    Acc8 acc; acc_zero(acc);
    float coef = 0.f;
    for (int it = 0; it < NIT - 2; ++it) {
      const char* pc = vb + itoff(it);
      const char* pn = vb + itoff(it + 1);
      const float pcur = sc[0];
      const int lvl = level(16 * TAUK + it * TAUV);
      static_for<0, 16>([&](auto j) {
        constexpr int J = j.v;
        wait_ld_lvl<(joff((J + D) & 15) & 4095)>(rs[J % R], rs[(J + D) % R], vo, ((J + D >= 16) ? pn : pc) + (joff((J + D) & 15) & ~4095), lvl);
        fma8(acc, dpp<BCAST + J>(pcur), rs[J % R]);
        SBAR();
      });
#pragma unroll
      for (int i = 0; i < NIT - 1; ++i) sc[i] = sc[i + 1];
    }
    {
      const char* pc = vb + itoff(NIT - 2);
      const char* pn = vb + itoff(NIT - 1);
      const float pcur = sc[0];
      lds_wait_count(lds, L_CNT + 1, 4u);
      if (lane == 0) __hip_atomic_fetch_add(reinterpret_cast<unsigned*>(lds) + L_VT + 6, 1u, RLX_WG);
      const int lvl = level(16 * TAUK + (NIT - 2) * TAUV);
      static_for<0, 16>([&](auto j) {
        constexpr int J = j.v;
        const u32x4 wt = *reinterpret_cast<const u32x4*>(&lds[L_WO + (r * 16 + J) * 256 + cb * 64 + c * 4]);
        wait_ld_lvl<(joff((J + D) & 15) & 4095)>(rs[J % R], rs[(J + D) % R], vo, ((J + D >= 16) ? pn : pc) + (joff((J + D) & 15) & ~4095), lvl);
        fma8(acc, dpp<BCAST + J>(pcur), rs[J % R]);
        unpack8(wf[J], wt);
        SBAR();
      });
    }
    {
      u32x2 lg;
      const gu64* lsep = (const gu64*)(a.ws + WS_LSE) + (b * NH + h) * 8 + (c & 7);
      asm volatile("global_load_dwordx2 %0, %1, off sc1" : "=v"(lg) : "v"(lsep) : "memory");
      const char* pc = vb + itoff(NIT - 1);
      const float pcur = sc[1];
      const int lvl = level(16 * TAUK + (NIT - 1) * TAUV);
      static_for<0, 16>([&](auto j) {
        constexpr int J = j.v;
        if constexpr (J + D < 16) wait_ld_lvl<(joff((J + D) & 15) & 4095)>(rs[J % R], rs[(J + D) % R], vo, pc + (joff((J + D) & 15) & ~4095), lvl);
        else wait_vm<15 - J>(rs[J % R]);
        if constexpr (J == D + 1) {
          asm volatile("" : "+v"(lg));
          unsigned lv = lg[0], lt = lg[1];
          for (unsigned spins = 0; spins < SPIN_CAP; ++spins) {
            if (__all(lt == tag)) break;
            __builtin_amdgcn_s_sleep(8);
            const unsigned long long v = __hip_atomic_load(lsep, RLX_AGENT);
            lv = (unsigned)v; lt = (unsigned)(v >> 32);
          }
          const float le = __uint_as_float(lv);
          const float M = row_max(le);
          const float L = 0.5f * row_sum(__builtin_amdgcn_exp2f(le - M));
          coef = __builtin_amdgcn_exp2f(mw - M) / L;
        }
        fma8(acc, dpp<BCAST + J>(pcur), rs[J % R]);
        SBAR();
      });
    }
    float u0, u1;
    xrow_reduce8(acc, u0, u1);
    *reinterpret_cast<f32x2*>(&lds[L_RED + w * 128 + 8 * c + 2 * r]) = f32x2{u0 * coef, u1 * coef};
  } else {
    const int hw = w - 4;
    const int cg = hh * 2 + s;
    const int f0 = xcd * 224 + (cg < 7 ? cg : 6) * 32;
    u32x4 xc; unsigned xo0, xo1;
    {
      const char* xb = a.x + b * (DM * 2);
      ld16<0>(xc, lane * 16, xb);
      asm volatile("global_load_ushort %0, %1, %2 nt" : "=v"(xo0) : "v"((unsigned)(lane * 2)), "s"(xb + hw * 256) : "memory");
      asm volatile("global_load_ushort %0, %1, %2 offset:128 nt" : "=v"(xo1) : "v"((unsigned)(lane * 2)), "s"(xb + hw * 256) : "memory");
    }
    const char* wqb = a.wq + (size_t)(hw * 128) * (NH * DH * 2) + h * (DH * 2);
    const unsigned voq = (unsigned)(r * 16) * (NH * DH * 2) + c * 16;
    u32x4 wt[32];
    static_for<0, HD>([&](auto t) { ld16<0>(wt[t.v], voq, wqb + ((t.v >> 4) * 64 + (t.v & 15)) * (NH * DH * 2)); });
    if (tid == 256) { lds[L_CNT] = 0.f; lds[L_CNT + 1] = 0.f; lds[L_VT + 5] = 0.f; lds[L_VT + 6] = 0.f; }
    asm volatile("s_waitcnt vmcnt(%3)" : "+v"(xc), "+v"(xo0), "+v"(xo1) : "i"(HD) : "memory");
    float xn[2];
    {
      float xv[8];
#pragma unroll
      for (int k = 0; k < 4; ++k) { xv[2 * k] = lo16(xc[k]); xv[2 * k + 1] = hi16(xc[k]); }
      float sm = ((xv[0] + xv[1]) + (xv[2] + xv[3])) + ((xv[4] + xv[5]) + (xv[6] + xv[7]));
      const float mean = wave_sum(sm) * (1.0f / DM);
      float sq = 0.f;
#pragma unroll
      for (int k = 0; k < 8; ++k) { const float d = xv[k] - mean; sq = fmaf(d, d, sq); }
      const float var = wave_sum(sq) * (1.0f / DM);
      const float rstd = 1.0f / sqrtf(var + 1e-5f);
      xn[0] = (lo16(xo0) - mean) * rstd;
      xn[1] = (lo16(xo1) - mean) * rstd;
    }
    {
      Acc8 acc; acc_zero(acc);
      SBAR();
      static_for<0, 32>([&](auto t) {
        constexpr int T = t.v, I = T + HD;
        wait_vm<(I <= 32 ? HD - 1 : 31 - T)>(wt[T]);
        if constexpr (I < 32) ld16<0>(wt[I], voq, wqb + ((I >> 4) * 64 + (I & 15)) * (NH * DH * 2));
        fma8(acc, dpp<BCAST + (T & 15)>(xn[T >> 4]), wt[T]);
        SBAR();
      });
      float u0, u1;
      xrow_reduce8(acc, u0, u1);
      *reinterpret_cast<f32x2*>(&lds[L_QP + hw * 128 + 8 * c + 2 * r]) = f32x2{u0, u1};
    }
    wg_barrier();
    if (hw == 1) {
      unsigned t0_, t1_, t2_, t3_, t4_;
      asm volatile("global_load_dword %0, %1, %2 nt" : "=v"(t0_) : "v"(0u), "s"(a.v + (size_t)(b * NH + h) * SK * (DH * 2)) : "memory");
      asm volatile("global_load_dword %0, %1, %2 nt" : "=v"(t1_) : "v"(0u), "s"(a.out + b * (DM * 2)) : "memory");
      asm volatile("global_load_dword %0, %1, %2 nt" : "=v"(t2_) : "v"(0u), "s"(a.wo + (size_t)(h * DH) * (DM * 2)) : "memory");
      asm volatile("global_load_dword %0, %1, %2 nt" : "=v"(t3_) : "v"(0u), "s"(a.wd + (size_t)f0 * (DM * 2)) : "memory");
      asm volatile("global_load_dword %0, %1, %2 nt" : "=v"(t4_) : "v"(0u), "s"(a.ws + WS_G + (size_t)g * 4096) : "memory");
      asm volatile("s_waitcnt vmcnt(0)" : "+v"(t0_), "+v"(t1_), "+v"(t2_), "+v"(t3_), "+v"(t4_) :: "memory");
    }
    if (hw == 0 && lane < 4) reinterpret_cast<unsigned*>(lds)[L_TAG + lane] = __hip_atomic_load((const gu32*)(a.ws + WS_EPOCH + lane * 256), RLX_AGENT) + 1u;
    {
      Acc8 ag, au; acc_zero(ag); acc_zero(au);
      const unsigned vog = (unsigned)((lane >> 2) * 4) * (FF * 2) + (lane & 3) * 16;
      const size_t sog = (size_t)(hw * 128) * (FF * 2) + f0 * 2;
      static_for<0, 8>([&](auto t) {
        constexpr int T = t.v;
        u32x4 gt, ut;
        ld16<0>(gt, vog, a.wg + sog + ((T >> 2) * 64 + (T & 3)) * (FF * 2));
        ld16<0>(ut, vog, a.wu + sog + ((T >> 2) * 64 + (T & 3)) * (FF * 2));
        const float xb_ = dpp<(T & 3) * 0x55>(xn[T >> 2]);
        wait_vm<1>(gt); fma8(ag, xb_, gt);
        wait_vm<0>(ut); fma8(au, xb_, ut);
        SBAR();
      });
      float t8[8];
#pragma unroll
      for (int k = 0; k < 8; ++k) t8[k] = swap32_add(ag.a[k >> 1][k & 1], au.a[k >> 1][k & 1]);
#pragma unroll
      for (int k = 0; k < 4; ++k) {
        float t = swap16_add(t8[k], t8[k + 4]); t += dpp<ROR4>(t); t += dpp<ROR8>(t);
        if ((lane & 12) == 0) lds[L_GU + ((r >> 1) * 32 + 8 * (lane & 3) + 4 * (r & 1) + k) * 4 + hw] = t;
      }
      lds_arrive(lds, L_CNT, lane);
      u32x4 wd[8];
      const char* wdb = a.wd + (size_t)f0 * (DM * 2) + hw * 256;
      const unsigned vod = (unsigned)(r * 8) * (DM * 2) + c * 16;
      static_for<0, 8>([&](auto t) {
        constexpr int T = t.v;
        if constexpr (T < 2) ld16<(T & 3) * 1024>(wd[T], vod, wdb + (T >> 2) * 4096);
        else pace_ld<1, (T & 3) * 1024>(wd[T], vod, wdb + (T >> 2) * 4096);
      });
      lds_wait_count(lds, L_CNT, 4u);
      const int f = r * 8 + (c & 7);
      const f32x4 g4 = *reinterpret_cast<const f32x4*>(&lds[L_GU + f * 4]), u4 = *reinterpret_cast<const f32x4*>(&lds[L_GU + (32 + f) * 4]);
      const float gs = (g4[0] + g4[1]) + (g4[2] + g4[3]), us = (u4[0] + u4[1]) + (u4[2] + u4[3]);
      const float hv = gs / (1.0f + __expf(-gs)) * us;
      Acc8 ay; acc_zero(ay);
      asm volatile("s_waitcnt vmcnt(0)" : "+v"(wd[0]), "+v"(wd[1]), "+v"(wd[2]), "+v"(wd[3]), "+v"(wd[4]), "+v"(wd[5]), "+v"(wd[6]), "+v"(wd[7]) :: "memory");
      if (cg < 7) static_for<0, 8>([&](auto t) { fma8(ay, dpp<BCAST + t.v>(hv), wd[t.v]); });
      float y0, y1;
      xrow_reduce8(ay, y0, y1);
      *reinterpret_cast<f32x2*>(&lds[L_YF + hw * 128 + 8 * c + 2 * r]) = f32x2{y0, y1};
    }
    {
      const char* wo0 = a.wo + (size_t)(h * DH + hw * 32) * (DM * 2);
      for (int i = 0; i < 32; i += 2) {
        u32x4 t0, t1;
        ld16<0>(t0, lane * 16, wo0 + i * 1024);
        ld16<1024>(t1, lane * 16, wo0 + i * 1024);
        wait_vm<1>(t0);
        *reinterpret_cast<u32x4*>(&lds[L_WO + (hw * 32 + i) * 256 + lane * 4]) = t0;
        wait_vm<0>(t1);
        *reinterpret_cast<u32x4*>(&lds[L_WO + (hw * 32 + i + 1) * 256 + lane * 4]) = t1;
      }
      lds_arrive(lds, L_CNT + 1, lane);
      lds_wait_count(lds, L_CNT + 1, 4u);
      static_for<0, 16>([&](auto t) { unpack8(wf[t.v], *reinterpret_cast<const u32x4*>(&lds[L_WO + (64 + r * 16 + t.v) * 256 + cb * 64 + c * 4])); });
    }
    tag = __builtin_amdgcn_readfirstlane(reinterpret_cast<const unsigned*>(lds)[L_TAG + b]);
    if (hw == 0) {
      unsigned* lv = reinterpret_cast<unsigned*>(lds) + L_VT;
      unsigned tg4[4];
#pragma unroll
      for (int i = 0; i < 4; ++i) tg4[i] = reinterpret_cast<const unsigned*>(lds)[L_TAG + i];
      gu64* pg = (gu64*)(a.ws + WS_PROG);
      gu64* pm = (gu64*)(a.ws + WS_MEAN);
      for (unsigned spins = 0; spins < (1u << 16); ++spins) {
        if (__builtin_amdgcn_readfirstlane(__hip_atomic_load(lv + 6, RLX_WG)) != 0u) break;
        const unsigned now = (unsigned)__builtin_amdgcn_s_memrealtime() >> 8;
        const unsigned v0 = __hip_atomic_load(lv, RLX_WG), v1 = __hip_atomic_load(lv + 1, RLX_WG), v2 = __hip_atomic_load(lv + 2, RLX_WG), v3 = __hip_atomic_load(lv + 3, RLX_WG);
        const unsigned v = v0 + (unsigned)(((int)(v1 - v0) + (int)(v2 - v0) + (int)(v3 - v0)) >> 2);
        if (lane == 0) __hip_atomic_store(pg + (now & 63u) * 256 + g, ((unsigned long long)tag << 32) | v, RLX_AGENT);
        if (g == 0) {
          float sd = 0.f, sn = 0.f;
          const gu64* pr = pg + ((now - 1u) & 63u) * 256 + lane;
#pragma unroll
          for (int i = 0; i < 4; ++i) {
            const unsigned long long x = __hip_atomic_load(pr + i * 64, RLX_AGENT);
            const bool ok = (unsigned)(x >> 32) == tg4[i];
            sd += ok ? (float)(int)((unsigned)x - v) : 0.f; sn += ok ? 1.f : 0.f;
          }
          sd = wave_sum(sd); sn = wave_sum(sn);
          if (lane < 32 && sn >= 128.f) __hip_atomic_store(pm + lane * 64, ((unsigned long long)tag << 32) | (v + (unsigned)(int)(sd / sn)), RLX_AGENT);
        }
        const unsigned long long m = __hip_atomic_load(pm + (g & 31) * 64, RLX_AGENT);
        if (lane == 0 && (unsigned)(m >> 32) == tg4[0]) { __hip_atomic_store(lv + 4, (unsigned)m, RLX_WG); __hip_atomic_store(lv + 5, 1u, RLX_WG); }
        __builtin_amdgcn_s_sleep(48);
      }
    }
  }
  wg_barrier();
  {
    float ov = 0.f;
#pragma unroll
    for (int i = 0; i < 4; ++i) ov += lds[L_RED + i * 128 + rh * 64 + lane];
    const f32x2 yf = *reinterpret_cast<const f32x2*>(&lds[L_YF + cb * 128 + 8 * c + 2 * r]);
    Acc8 ay; acc_zero(ay);
    static_for<0, 16>([&](auto t) {
      const float o1 = dpp<BCAST + t.v>(ov);
      const f32x2 o2 = {o1, o1};
#pragma unroll
      for (int k = 0; k < 4; ++k) ay.a[k] = __builtin_elementwise_fma(o2, wf[t.v].a[k], ay.a[k]);
    });
    float y0, y1;
    xrow_reduce8(ay, y0, y1);
    if (rh == 1) { y0 += yf[0]; y1 += yf[1]; }
    lds[L_QP + w * 64 + lane] = rh ? y0 : y1;
    wg_barrier();
    const float yo = lds[L_QP + (w ^ 4) * 64 + lane];
    const float ys = (rh ? y1 : y0) + yo;
    gu64* gp = (gu64*)(a.ws + WS_G) + ((size_t)(b * 64 + pidx)) * 512 + (cb * 2 + rh) * 64 + lane;
    __hip_atomic_store(gp, ((unsigned long long)tag << 32) | __float_as_uint(ys), RLX_AGENT);
  }
  if (w != 0) return;
  {
    const int P = pidx * 8 + (lane & 7), pl = P & 63;
    const int col = (P >> 7) * 128 + 8 * (pl & 15) + 2 * (pl >> 4) + ((P >> 6) & 1);
    const gu64* gp = (const gu64*)(a.ws + WS_G) + (size_t)(b * 64 + (lane >> 3)) * 512 + P;
    const float xv = lo16(__builtin_nontemporal_load(reinterpret_cast<const unsigned short*>(a.x) + b * DM + col));
    unsigned val[8];
    bool ok = false;
    __builtin_amdgcn_s_sleep(24);
    for (unsigned spins = 0; spins < SPIN_CAP; ++spins) {
      if (!ok) {
        bool o = true;
#pragma unroll
        for (int i = 0; i < 8; ++i) { const unsigned long long v = __hip_atomic_load(gp + i * 4096, RLX_AGENT); val[i] = (unsigned)v; o &= (unsigned)(v >> 32) == tag; }
        ok = o;
      }
      if (__all(ok)) break;
      __builtin_amdgcn_s_sleep(4);
    }
    float t = 0.f;
#pragma unroll
    for (int i = 0; i < 8; ++i) t += __uint_as_float(val[i]);
    t += dpp<ROR8>(t);
    t = swap16_add(t, t);
    t = swap32_add(t, t);
    if (lane < 8) reinterpret_cast<__bf16*>(a.out)[b * DM + col] = (__bf16)(xv + t);
    if (pidx == 0 && lane == 0) __hip_atomic_store((gu32*)(a.ws + WS_EPOCH + b * 256), tag, RLX_AGENT);
  }
}

extern "C" void kernel_launch(void* const* d_in, const int* in_sizes, int n_in, void* d_out, int out_size, void* d_ws, size_t ws_size,
                              hipStream_t stream) {
  Args a;
  a.x = (const char*)d_in[0]; a.k = (const char*)d_in[1]; a.v = (const char*)d_in[2]; a.wq = (const char*)d_in[3]; a.wo = (const char*)d_in[4];
  a.wg = (const char*)d_in[6]; a.wu = (const char*)d_in[7]; a.wd = (const char*)d_in[8];
  a.out = (char*)d_out; a.ws = (char*)d_ws;
  hipLaunchKernelGGL(fused_decode_kernel, dim3(NWG), dim3(NTHR), 0, stream, a);
}
```

```cpp
#include <hip/hip_runtime.h>
#include <hip/hip_bf16.h>
#include <stdint.h>

typedef unsigned u32x4 __attribute__((ext_vector_type(4)));
typedef unsigned u32x2 __attribute__((ext_vector_type(2)));
typedef float f32x2 __attribute__((ext_vector_type(2)));
typedef float f32x4 __attribute__((ext_vector_type(4)));
typedef __attribute__((ext_vector_type(2))) __bf16 bf16x2_t;
#define GAS __attribute__((address_space(1)))
typedef GAS unsigned gu32;
typedef GAS unsigned long long gu64;
#define RLX_AGENT __ATOMIC_RELAXED, __HIP_MEMORY_SCOPE_AGENT
#define RLX_WG __ATOMIC_RELAXED, __HIP_MEMORY_SCOPE_WORKGROUP

constexpr int DM = 512, NH = 32, SK = 8192, DH = 128, FF = 1792;
constexpr int NWG = 256, NTHR = 512;
constexpr int D = 8, DMID = 6, DLO = 4;
constexpr int R = 16;
constexpr int A = 50;
constexpr int HD = 8;
constexpr int NIT = 16;
constexpr int NUN = ((2 * (A - D) + 15) / 16) * 16;
constexpr int TAUK = 228, TAUV = 222, TMARG = 30;
constexpr unsigned SPIN_CAP = 1u << 18;
static_assert(D < R && NUN <= 16 * (NIT - 1), "schedule");
constexpr int joff(int J) { return (J >> 2) * 16384 + (J & 3) * 1024; }
__device__ constexpr size_t itoff(int it) { return (size_t)it << 17; }

constexpr size_t WS_EPOCH = 0;
constexpr size_t WS_LSE = 4096;
constexpr size_t WS_MEAN = 32768;
constexpr size_t WS_PROG = 65536;
constexpr size_t WS_G = 262144;

constexpr int L_QP = 0;
constexpr int L_RED = 512;
constexpr int L_GU = 1024;
constexpr int L_YF = 1280;
constexpr int L_CNT = 1792;
constexpr int L_VT = 1800;
constexpr int L_TAG = 1808;
constexpr int L_WO = 2048;
constexpr int L_FLOATS = L_WO + 32768;

template <int OFF> __device__ __forceinline__ void ld16(u32x4& d, unsigned voff, const void* sb) {
  asm volatile("global_load_dwordx4 %0, %1, %2 offset:%3 nt" : "=v"(d) : "v"(voff), "s"(sb), "i"(OFF) : "memory");
}
template <int N, int OFF> __device__ __forceinline__ void pace_ld(u32x4& nxt, unsigned voff, const void* sb) {
  asm volatile("s_waitcnt vmcnt(%3)\n\tglobal_load_dwordx4 %0, %1, %2 offset:%4 nt" : "=v"(nxt) : "v"(voff), "s"(sb), "i"(N), "i"(OFF) : "memory");
}
template <int N, int OFF> __device__ __forceinline__ void wait_ld(u32x4& cur, u32x4& nxt, unsigned voff, const void* sb) {
  asm volatile("s_waitcnt vmcnt(%4)\n\tglobal_load_dwordx4 %1, %2, %3 offset:%5 nt" : "+v"(cur), "+v"(nxt) : "v"(voff), "s"(sb), "i"(N), "i"(OFF) : "memory");
}
template <int OFF> __device__ __forceinline__ void wait_ld_lvl(u32x4& cur, u32x4& nxt, unsigned voff, const void* sb, int lvl) {
  asm volatile("s_cmp_lt_i32 %4, 1\n\ts_cbranch_scc1 .Lthr%=\n\ts_waitcnt vmcnt(%6)\n\ts_cmp_lt_i32 %4, 2\n\ts_cbranch_scc1 .Lthr%=\n\ts_waitcnt vmcnt(%7)\n.Lthr%=:\n\t"
               "s_waitcnt vmcnt(%5)\n\tglobal_load_dwordx4 %1, %2, %3 offset:%8 nt"
               : "+v"(cur), "+v"(nxt) : "v"(voff), "s"(sb), "s"(lvl), "i"(D - 1), "i"(DMID - 1), "i"(DLO - 1), "i"(OFF) : "memory", "scc");
}
#define SBAR() __builtin_amdgcn_sched_barrier(0)
template <int N> __device__ __forceinline__ void wait_vm(u32x4& r) { asm volatile("s_waitcnt vmcnt(%1)" : "+v"(r) : "i"(N) : "memory"); }
__device__ __forceinline__ void landed(u32x4& r) { asm volatile("" : "+v"(r)); }
__device__ __forceinline__ void wg_barrier() { asm volatile("s_waitcnt lgkmcnt(0)\n\ts_barrier" ::: "memory"); }

template <int CTRL> __device__ __forceinline__ float dpp(float x) {
  return __builtin_bit_cast(float, __builtin_amdgcn_mov_dpp(__builtin_bit_cast(int, x), CTRL, 0xf, 0xf, true));
}
constexpr int XOR1 = 0xB1, XOR2 = 0x4E, HMIR = 0x141, RMIR = 0x140, ROR4 = 0x124, ROR8 = 0x128, BCAST = 0x150;
__device__ __forceinline__ float lo16(unsigned u) { return __builtin_bit_cast(float, u << 16); }
__device__ __forceinline__ float hi16(unsigned u) { return __builtin_bit_cast(float, u & 0xffff0000u); }
__device__ __forceinline__ float dot2(unsigned k, unsigned q, float acc) {
  return __builtin_amdgcn_fdot2_f32_bf16(__builtin_bit_cast(bf16x2_t, k), __builtin_bit_cast(bf16x2_t, q), acc, false);
}
__device__ __forceinline__ unsigned pack_bf16(float lo, float hi) {
  bf16x2_t v = {(__bf16)lo, (__bf16)hi};
  return __builtin_bit_cast(unsigned, v);
}
__device__ __forceinline__ float swap32_add(float a, float b) {
  auto s = __builtin_amdgcn_permlane32_swap(__float_as_uint(a), __float_as_uint(b), false, false);
  return __uint_as_float(s[0]) + __uint_as_float(s[1]);
}
__device__ __forceinline__ float swap16_add(float a, float b) {
  auto s = __builtin_amdgcn_permlane16_swap(__float_as_uint(a), __float_as_uint(b), false, false);
  return __uint_as_float(s[0]) + __uint_as_float(s[1]);
}
__device__ __forceinline__ float swap32_max(float a) {
  auto s = __builtin_amdgcn_permlane32_swap(__float_as_uint(a), __float_as_uint(a), false, false);
  return fmaxf(__uint_as_float(s[0]), __uint_as_float(s[1]));
}
__device__ __forceinline__ float swap16_max(float a) {
  auto s = __builtin_amdgcn_permlane16_swap(__float_as_uint(a), __float_as_uint(a), false, false);
  return fmaxf(__uint_as_float(s[0]), __uint_as_float(s[1]));
}
__device__ __forceinline__ float row_sum(float x) { x += dpp<XOR1>(x); x += dpp<XOR2>(x); x += dpp<HMIR>(x); x += dpp<RMIR>(x); return x; }
__device__ __forceinline__ float row_max(float x) {
  x = fmaxf(x, dpp<XOR1>(x)); x = fmaxf(x, dpp<XOR2>(x)); x = fmaxf(x, dpp<HMIR>(x)); x = fmaxf(x, dpp<RMIR>(x)); return x;
}
__device__ __forceinline__ float wave_sum(float x) { x = row_sum(x); x = swap16_add(x, x); return swap32_add(x, x); }
__device__ __forceinline__ float wave_max(float x) { x = row_max(x); x = swap16_max(x); return swap32_max(x); }
struct Acc8 { f32x2 a[4]; };
__device__ __forceinline__ void acc_zero(Acc8& acc) {
#pragma unroll
  for (int k = 0; k < 4; ++k) acc.a[k] = f32x2{0.f, 0.f};
}
__device__ __forceinline__ void pin(Acc8& acc) { asm volatile("" : "+v"(acc.a[0]), "+v"(acc.a[1]), "+v"(acc.a[2]), "+v"(acc.a[3])); }
__device__ __forceinline__ void fma8(Acc8& acc, float s, const u32x4& w) {
  const f32x2 s2 = {s, s};
#pragma unroll
  for (int k = 0; k < 4; ++k) acc.a[k] = __builtin_elementwise_fma(s2, f32x2{lo16(w[k]), hi16(w[k])}, acc.a[k]);
  pin(acc);
}
__device__ __forceinline__ void unpack8(Acc8& o, const u32x4& w) {
#pragma unroll
  for (int k = 0; k < 4; ++k) o.a[k] = f32x2{lo16(w[k]), hi16(w[k])};
  pin(o);
}
__device__ __forceinline__ void xrow_reduce8(const Acc8& x, float& u0, float& u1) {
  const float t0 = swap32_add(x.a[0][0], x.a[2][0]), t1 = swap32_add(x.a[0][1], x.a[2][1]), t2 = swap32_add(x.a[1][0], x.a[3][0]), t3 = swap32_add(x.a[1][1], x.a[3][1]);
  u0 = swap16_add(t0, t2); u1 = swap16_add(t1, t3);
}
template <int T> struct IC { static constexpr int v = T; };
template <int I, int N, class F> __device__ __forceinline__ void static_for(F&& f) { if constexpr (I < N) { f(IC<I>{}); static_for<I + 1, N>(f); } }
__device__ __forceinline__ void lds_wait_count(float* lds, int idx, unsigned want) {
  for (unsigned spins = 0; spins < (SPIN_CAP << 2); ++spins) {
    const unsigned cnt = __hip_atomic_load(reinterpret_cast<unsigned*>(lds) + idx, RLX_WG);
    if (__builtin_amdgcn_readfirstlane(cnt) >= want) break;
    __builtin_amdgcn_s_sleep(1);
  }
  asm volatile("" ::: "memory");
}
__device__ __forceinline__ void lds_arrive(float* lds, int idx, int lane) {
  asm volatile("s_waitcnt lgkmcnt(0)" ::: "memory");
  if (lane == 0) __hip_atomic_fetch_add(reinterpret_cast<unsigned*>(lds) + idx, 1u, RLX_WG);
}
constexpr int cu_issued(int n) { return n < 2 * (A - D) ? A + n / 2 + 1 : n + D + 1; }

struct Args {
  const char* x; const char* k; const char* v; const char* wq; const char* wo; const char* wg; const char* wu; const char* wd;
  char* out; char* ws;
};

__global__ void __launch_bounds__(NTHR, 2) fused_decode_kernel(Args a) {
  __shared__ __attribute__((aligned(16))) float lds[L_FLOATS];
  const int tid = threadIdx.x, lane = tid & 63, w = __builtin_amdgcn_readfirstlane(tid >> 6);
  const int g = blockIdx.x, xcd = g & 7;
  const int s = (g >> 3) & 1, hh = (g >> 4) & 3, b = g >> 6, h = hh * 8 + xcd;
  const int r = lane >> 4, c = lane & 15;
  const int pidx = h * 2 + s;
  const int cb = w & 3, rh = w >> 2;
  Acc8 wf[16];
  unsigned tag;

  if (w < 4) {
    auto level = [&](int sched) -> int {
      const unsigned V = (unsigned)__builtin_amdgcn_s_memrealtime() - (unsigned)sched;
      unsigned* lv = reinterpret_cast<unsigned*>(lds) + L_VT;
      __hip_atomic_store(lv + w, V, RLX_WG);
      const unsigned vb_ = __hip_atomic_load(lv + 4, RLX_WG), ok = __hip_atomic_load(lv + 5, RLX_WG);
      const int d = (int)(V - vb_);
      return __builtin_amdgcn_readfirstlane(ok == 0u ? 1 : (d < -TMARG ? 2 : (d < TMARG ? 1 : 0)));
    };
    const size_t kvoff = (size_t)(b * NH + h) * SK * (DH * 2) + ((size_t)s << 16) + w * 4096;
    const char* kb = a.k + kvoff;
    const char* vb = a.v + kvoff;
    const unsigned vo = lane * 16;
    u32x4 win[NUN];
    u32x4 rs[R];
#pragma unroll
    for (int i = 0; i < R; ++i) rs[i] = u32x4{0u, 0u, 0u, 0u};
    static_for<0, A>([&](auto i) {
      constexpr int I = i.v;
      if constexpr (I < DMID) ld16<(joff(I & 15) & 4095)>(win[I], vo, kb + itoff(I >> 4) + (joff(I & 15) & ~4095));
      else pace_ld<DMID - 1, (joff(I & 15) & 4095)>(win[I], vo, kb + itoff(I >> 4) + (joff(I & 15) & ~4095));
    });
    wg_barrier();
    unsigned qh[4], ql[4];
    {
      float q0 = 0.f, q1 = 0.f;
#pragma unroll
      for (int i = 0; i < 4; ++i) { const f32x2 t = *reinterpret_cast<const f32x2*>(&lds[L_QP + i * 128 + 8 * c + 2 * r]); q0 += t[0]; q1 += t[1]; }
      constexpr float QS = 0.08838834764831845f * 1.4426950408889634f;
      q0 *= QS; q1 *= QS;
      const unsigned ph = pack_bf16(q0, q1);
      const unsigned pl = pack_bf16(q0 - lo16(ph), q1 - hi16(ph));
      {
        auto s1 = __builtin_amdgcn_permlane16_swap(ph, ph, false, false);
        auto e = __builtin_amdgcn_permlane32_swap(s1[0], s1[0], false, false);
        auto o = __builtin_amdgcn_permlane32_swap(s1[1], s1[1], false, false);
        qh[0] = e[0]; qh[2] = e[1]; qh[1] = o[0]; qh[3] = o[1];
      }
      {
        auto s1 = __builtin_amdgcn_permlane16_swap(pl, pl, false, false);
        auto e = __builtin_amdgcn_permlane32_swap(s1[0], s1[0], false, false);
        auto o = __builtin_amdgcn_permlane32_swap(s1[1], s1[1], false, false);
        ql[0] = e[0]; ql[2] = e[1]; ql[1] = o[0]; ql[3] = o[1];
      }
    }
    float sc[NIT];
#pragma unroll
    for (int i = 0; i < NIT; ++i) sc[i] = 0.f;
    const bool b0 = (c & 1) != 0, b1 = (c & 2) != 0;
    const int cq = c >> 2;
    auto reduce4 = [&](const float (&p)[4]) -> float {
      const float k0 = b0 ? p[1] : p[0], s0 = b0 ? p[0] : p[1];
      const float k1 = b0 ? p[3] : p[2], s1 = b0 ? p[2] : p[3];
      const float a0 = k0 + dpp<XOR1>(s0), a1 = k1 + dpp<XOR1>(s1);
      const float k2 = b1 ? a1 : a0, s2 = b1 ? a0 : a1;
      float t = k2 + dpp<XOR2>(s2);
      t += dpp<ROR4>(t); t += dpp<ROR8>(t);
      return t;
    };
    auto kdot = [&](const u32x4& kv) -> float {
      float d0 = dot2(kv[0], qh[0], 0.f), d1 = dot2(kv[0], ql[0], 0.f);
#pragma unroll
      for (int k = 1; k < 4; ++k) { d0 = dot2(kv[k], qh[k], d0); d1 = dot2(kv[k], ql[k], d1); }
      return d0 + d1;
    };
    {
      float part[4], snew = 0.f;
      static_for<0, NUN>([&](auto j) {
        constexpr int N = j.v;
        constexpr int I = cu_issued(N) - 1;
        constexpr bool issues = (N == 0) || (cu_issued(N) != cu_issued(N - 1));
        if constexpr (issues) {
          if constexpr (I < NUN) pace_ld<DMID - 1, (joff(I & 15) & 4095)>(win[I], vo, kb + itoff(I >> 4) + (joff(I & 15) & ~4095));
          else wait_ld<DMID - 1, (joff(I & 15) & 4095)>(win[N], rs[I % R], vo, kb + itoff(I >> 4) + (joff(I & 15) & ~4095));
        }
        landed(win[N]);
        part[N & 3] = kdot(win[N]);
        if constexpr ((N & 3) == 3) { const float t = reduce4(part); snew = (cq == ((N & 15) >> 2)) ? t : snew; }
        if constexpr ((N & 15) == 15) sc[NIT - NUN / 16 + N / 16] = snew;
        SBAR();
      });
    }
    for (int it = NUN / 16; it < NIT; ++it) {
      const char* pc = kb + itoff(it);
      const char* pn = it < NIT - 1 ? kb + itoff(it + 1) : vb;
      float part[4], snew = 0.f;
      const int lvl = level(it * TAUK);
      static_for<0, 16>([&](auto j) {
        constexpr int J = j.v;
        wait_ld_lvl<(joff((J + D) & 15) & 4095)>(rs[J % R], rs[(J + D) % R], vo, ((J + D >= 16) ? pn : pc) + (joff((J + D) & 15) & ~4095), lvl);
        part[J & 3] = kdot(rs[J % R]);
        if constexpr ((J & 3) == 3) { const float t = reduce4(part); snew = (cq == (J >> 2)) ? t : snew; }
        SBAR();
      });
#pragma unroll
      for (int i = 0; i < NIT - 1; ++i) sc[i] = sc[i + 1];
      sc[NIT - 1] = snew;
    }
    lds_wait_count(lds, L_CNT, 4u);
    tag = __builtin_amdgcn_readfirstlane(reinterpret_cast<const unsigned*>(lds)[L_TAG + b]);
    float mw;
    {
      float m = sc[0];
#pragma unroll
      for (int i = 1; i < NIT; ++i) m = fmaxf(m, sc[i]);
      mw = wave_max(m);
      float l = 0.f;
#pragma unroll
      for (int i = 0; i < NIT; ++i) { sc[i] = __builtin_amdgcn_exp2f(sc[i] - mw); l += sc[i]; }
      l = wave_sum(l);
      const float lse = mw + __builtin_amdgcn_logf(l);
      if (lane == 0)
        __hip_atomic_store((gu64*)(a.ws + WS_LSE) + (b * NH + h) * 8 + s * 4 + w, ((unsigned long long)tag << 32) | __float_as_uint(lse), RLX_AGENT);
    }
    Acc8 acc; acc_zero(acc);
    float coef = 0.f;
    for (int it = 0; it < NIT - 2; ++it) {
      const char* pc = vb + itoff(it);
      const char* pn = vb + itoff(it + 1);
      const float pcur = sc[0];
      const int lvl = level(16 * TAUK + it * TAUV);
      static_for<0, 16>([&](auto j) {
        constexpr int J = j.v;
        wait_ld_lvl<(joff((J + D) & 15) & 4095)>(rs[J % R], rs[(J + D) % R], vo, ((J + D >= 16) ? pn : pc) + (joff((J + D) & 15) & ~4095), lvl);
        fma8(acc, dpp<BCAST + J>(pcur), rs[J % R]);
        SBAR();
      });
#pragma unroll
      for (int i = 0; i < NIT - 1; ++i) sc[i] = sc[i + 1];
    }
    {
      const char* pc = vb + itoff(NIT - 2);
      const char* pn = vb + itoff(NIT - 1);
      const float pcur = sc[0];
      lds_wait_count(lds, L_CNT + 1, 4u);
      if (lane == 0) __hip_atomic_fetch_add(reinterpret_cast<unsigned*>(lds) + L_VT + 6, 1u, RLX_WG);
      const int lvl = level(16 * TAUK + (NIT - 2) * TAUV);
      static_for<0, 16>([&](auto j) {
        constexpr int J = j.v;
        const u32x4 wt = *reinterpret_cast<const u32x4*>(&lds[L_WO + (r * 16 + J) * 256 + cb * 64 + c * 4]);
        wait_ld_lvl<(joff((J + D) & 15) & 4095)>(rs[J % R], rs[(J + D) % R], vo, ((J + D >= 16) ? pn : pc) + (joff((J + D) & 15) & ~4095), lvl);
        fma8(acc, dpp<BCAST + J>(pcur), rs[J % R]);
        unpack8(wf[J], wt);
        SBAR();
      });
    }
    {
      u32x2 lg;
      const gu64* lsep = (const gu64*)(a.ws + WS_LSE) + (b * NH + h) * 8 + (c & 7);
      asm volatile("global_load_dwordx2 %0, %1, off sc1" : "=v"(lg) : "v"(lsep) : "memory");
      const char* pc = vb + itoff(NIT - 1);
      const float pcur = sc[1];
      const int lvl = level(16 * TAUK + (NIT - 1) * TAUV);
      static_for<0, 16>([&](auto j) {
        constexpr int J = j.v;
        if constexpr (J + D < 16) wait_ld_lvl<(joff((J + D) & 15) & 4095)>(rs[J % R], rs[(J + D) % R], vo, pc + (joff((J + D) & 15) & ~4095), lvl);
        else wait_vm<15 - J>(rs[J % R]);
        if constexpr (J == D + 1) {
          asm volatile("" : "+v"(lg));
          unsigned lv = lg[0], lt = lg[1];
          for (unsigned spins = 0; spins < SPIN_CAP; ++spins) {
            if (__all(lt == tag)) break;
            __builtin_amdgcn_s_sleep(8);
            const unsigned long long v = __hip_atomic_load(lsep, RLX_AGENT);
            lv = (unsigned)v; lt = (unsigned)(v >> 32);
          }
          const float le = __uint_as_float(lv);
          const float M = row_max(le);
          const float L = 0.5f * row_sum(__builtin_amdgcn_exp2f(le - M));
          coef = __builtin_amdgcn_exp2f(mw - M) / L;
        }
        fma8(acc, dpp<BCAST + J>(pcur), rs[J % R]);
        SBAR();
      });
    }
    float u0, u1;
    xrow_reduce8(acc, u0, u1);
    *reinterpret_cast<f32x2*>(&lds[L_RED + w * 128 + 8 * c + 2 * r]) = f32x2{u0 * coef, u1 * coef};
  } else {
    const int hw = w - 4;
    const int cg = hh * 2 + s;
    const int f0 = xcd * 224 + (cg < 7 ? cg : 6) * 32;
    u32x4 xc; unsigned xo0, xo1;
    {
      const char* xb = a.x + b * (DM * 2);
      ld16<0>(xc, lane * 16, xb);
      asm volatile("global_load_ushort %0, %1, %2 nt" : "=v"(xo0) : "v"((unsigned)(lane * 2)), "s"(xb + hw * 256) : "memory");
      asm volatile("global_load_ushort %0, %1, %2 offset:128 nt" : "=v"(xo1) : "v"((unsigned)(lane * 2)), "s"(xb + hw * 256) : "memory");
    }
    const char* wqb = a.wq + (size_t)(hw * 128) * (NH * DH * 2) + h * (DH * 2);
    const unsigned voq = (unsigned)(r * 16) * (NH * DH * 2) + c * 16;
    u32x4 wt[32];
    static_for<0, HD>([&](auto t) { ld16<0>(wt[t.v], voq, wqb + ((t.v >> 4) * 64 + (t.v & 15)) * (NH * DH * 2)); });
    if (tid == 256) { lds[L_CNT] = 0.f; lds[L_CNT + 1] = 0.f; lds[L_VT + 5] = 0.f; lds[L_VT + 6] = 0.f; }
    asm volatile("s_waitcnt vmcnt(%3)" : "+v"(xc), "+v"(xo0), "+v"(xo1) : "i"(HD) : "memory");
    float xn[2];
    {
      float xv[8];
#pragma unroll
      for (int k = 0; k < 4; ++k) { xv[2 * k] = lo16(xc[k]); xv[2 * k + 1] = hi16(xc[k]); }
      float sm = ((xv[0] + xv[1]) + (xv[2] + xv[3])) + ((xv[4] + xv[5]) + (xv[6] + xv[7]));
      const float mean = wave_sum(sm) * (1.0f / DM);
      float sq = 0.f;
#pragma unroll
      for (int k = 0; k < 8; ++k) { const float d = xv[k] - mean; sq = fmaf(d, d, sq); }
      const float var = wave_sum(sq) * (1.0f / DM);
      const float rstd = 1.0f / sqrtf(var + 1e-5f);
      xn[0] = (lo16(xo0) - mean) * rstd;
      xn[1] = (lo16(xo1) - mean) * rstd;
    }
    {
      Acc8 acc; acc_zero(acc);
      SBAR();
      static_for<0, 32>([&](auto t) {
        constexpr int T = t.v, I = T + HD;
        wait_vm<(I <= 32 ? HD - 1 : 31 - T)>(wt[T]);
        if constexpr (I < 32) ld16<0>(wt[I], voq, wqb + ((I >> 4) * 64 + (I & 15)) * (NH * DH * 2));
        fma8(acc, dpp<BCAST + (T & 15)>(xn[T >> 4]), wt[T]);
        SBAR();
      });
      float u0, u1;
      xrow_reduce8(acc, u0, u1);
      *reinterpret_cast<f32x2*>(&lds[L_QP + hw * 128 + 8 * c + 2 * r]) = f32x2{u0, u1};
    }
    wg_barrier();
    if (hw == 0 && lane < 4) reinterpret_cast<unsigned*>(lds)[L_TAG + lane] = __hip_atomic_load((const gu32*)(a.ws + WS_EPOCH + lane * 256), RLX_AGENT) + 1u;
    {
      Acc8 ag, au; acc_zero(ag); acc_zero(au);
      const unsigned vog = (unsigned)((lane >> 2) * 4) * (FF * 2) + (lane & 3) * 16;
      const size_t sog = (size_t)(hw * 128) * (FF * 2) + f0 * 2;
      static_for<0, 8>([&](auto t) {
        constexpr int T = t.v;
        u32x4 gt, ut;
        ld16<0>(gt, vog, a.wg + sog + ((T >> 2) * 64 + (T & 3)) * (FF * 2));
        ld16<0>(ut, vog, a.wu + sog + ((T >> 2) * 64 + (T & 3)) * (FF * 2));
        const float xb_ = dpp<(T & 3) * 0x55>(xn[T >> 2]);
        wait_vm<1>(gt); fma8(ag, xb_, gt);
        wait_vm<0>(ut); fma8(au, xb_, ut);
        SBAR();
      });
      float t8[8];
#pragma unroll
      for (int k = 0; k < 8; ++k) t8[k] = swap32_add(ag.a[k >> 1][k & 1], au.a[k >> 1][k & 1]);
#pragma unroll
      for (int k = 0; k < 4; ++k) {
        float t = swap16_add(t8[k], t8[k + 4]); t += dpp<ROR4>(t); t += dpp<ROR8>(t);
        if ((lane & 12) == 0) lds[L_GU + ((r >> 1) * 32 + 8 * (lane & 3) + 4 * (r & 1) + k) * 4 + hw] = t;
      }
      lds_arrive(lds, L_CNT, lane);
      u32x4 wd[8];
      const char* wdb = a.wd + (size_t)f0 * (DM * 2) + hw * 256;
      const unsigned vod = (unsigned)(r * 8) * (DM * 2) + c * 16;
      static_for<0, 8>([&](auto t) {
        constexpr int T = t.v;
        if constexpr (T < 2) ld16<(T & 3) * 1024>(wd[T], vod, wdb + (T >> 2) * 4096);
        else pace_ld<1, (T & 3) * 1024>(wd[T], vod, wdb + (T >> 2) * 4096);
      });
      lds_wait_count(lds, L_CNT, 4u);
      const int f = r * 8 + (c & 7);
      const f32x4 g4 = *reinterpret_cast<const f32x4*>(&lds[L_GU + f * 4]), u4 = *reinterpret_cast<const f32x4*>(&lds[L_GU + (32 + f) * 4]);
      const float gs = (g4[0] + g4[1]) + (g4[2] + g4[3]), us = (u4[0] + u4[1]) + (u4[2] + u4[3]);
      const float hv = gs / (1.0f + __expf(-gs)) * us;
      Acc8 ay; acc_zero(ay);
      asm volatile("s_waitcnt vmcnt(0)" : "+v"(wd[0]), "+v"(wd[1]), "+v"(wd[2]), "+v"(wd[3]), "+v"(wd[4]), "+v"(wd[5]), "+v"(wd[6]), "+v"(wd[7]) :: "memory");
      if (cg < 7) static_for<0, 8>([&](auto t) { fma8(ay, dpp<BCAST + t.v>(hv), wd[t.v]); });
      float y0, y1;
      xrow_reduce8(ay, y0, y1);
      *reinterpret_cast<f32x2*>(&lds[L_YF + hw * 128 + 8 * c + 2 * r]) = f32x2{y0, y1};
    }
    {
      const char* wo0 = a.wo + (size_t)(h * DH + hw * 32) * (DM * 2);
      for (int i = 0; i < 32; i += 2) {
        u32x4 t0, t1;
        ld16<0>(t0, lane * 16, wo0 + i * 1024);
        ld16<1024>(t1, lane * 16, wo0 + i * 1024);
        wait_vm<1>(t0);
        *reinterpret_cast<u32x4*>(&lds[L_WO + (hw * 32 + i) * 256 + lane * 4]) = t0;
        wait_vm<0>(t1);
        *reinterpret_cast<u32x4*>(&lds[L_WO + (hw * 32 + i + 1) * 256 + lane * 4]) = t1;
      }
      lds_arrive(lds, L_CNT + 1, lane);
      lds_wait_count(lds, L_CNT + 1, 4u);
      static_for<0, 16>([&](auto t) { unpack8(wf[t.v], *reinterpret_cast<const u32x4*>(&lds[L_WO + (64 + r * 16 + t.v) * 256 + cb * 64 + c * 4])); });
    }
    if (hw == 1) {
      unsigned t0_;
      asm volatile("global_load_dword %0, %1, %2 nt" : "=v"(t0_) : "v"(0u), "s"(a.out + b * (DM * 2)) : "memory");
      asm volatile("s_waitcnt vmcnt(0)" : "+v"(t0_) :: "memory");
    }
    tag = __builtin_amdgcn_readfirstlane(reinterpret_cast<const unsigned*>(lds)[L_TAG + b]);
    if (hw == 0) {
      unsigned* lv = reinterpret_cast<unsigned*>(lds) + L_VT;
      unsigned tg4[4];
#pragma unroll
      for (int i = 0; i < 4; ++i) tg4[i] = reinterpret_cast<const unsigned*>(lds)[L_TAG + i];
      gu64* pg = (gu64*)(a.ws + WS_PROG);
      gu64* pm = (gu64*)(a.ws + WS_MEAN);
      for (unsigned spins = 0; spins < (1u << 16); ++spins) {
        if (__builtin_amdgcn_readfirstlane(__hip_atomic_load(lv + 6, RLX_WG)) != 0u) break;
        const unsigned now = (unsigned)__builtin_amdgcn_s_memrealtime() >> 8;
        const unsigned v0 = __hip_atomic_load(lv, RLX_WG), v1 = __hip_atomic_load(lv + 1, RLX_WG), v2 = __hip_atomic_load(lv + 2, RLX_WG), v3 = __hip_atomic_load(lv + 3, RLX_WG);
        const unsigned v = v0 + (unsigned)(((int)(v1 - v0) + (int)(v2 - v0) + (int)(v3 - v0)) >> 2);
        if (lane == 0) __hip_atomic_store(pg + (now & 63u) * 256 + g, ((unsigned long long)tag << 32) | v, RLX_AGENT);
        if (g == 0) {
          float sd = 0.f, sn = 0.f;
          const gu64* pr = pg + ((now - 1u) & 63u) * 256 + lane;
#pragma unroll
          for (int i = 0; i < 4; ++i) {
            const unsigned long long x = __hip_atomic_load(pr + i * 64, RLX_AGENT);
            const bool ok = (unsigned)(x >> 32) == tg4[i];
            sd += ok ? (float)(int)((unsigned)x - v) : 0.f; sn += ok ? 1.f : 0.f;
          }
          sd = wave_sum(sd); sn = wave_sum(sn);
          if (lane < 32 && sn >= 128.f) __hip_atomic_store(pm + lane * 64, ((unsigned long long)tag << 32) | (v + (unsigned)(int)(sd / sn)), RLX_AGENT);
        }
        const unsigned long long m = __hip_atomic_load(pm + (g & 31) * 64, RLX_AGENT);
        if (lane == 0 && (unsigned)(m >> 32) == tg4[0]) { __hip_atomic_store(lv + 4, (unsigned)m, RLX_WG); __hip_atomic_store(lv + 5, 1u, RLX_WG); }
        __builtin_amdgcn_s_sleep(48);
      }
    }
  }
  wg_barrier();
  {
    float ov = 0.f;
#pragma unroll
    for (int i = 0; i < 4; ++i) ov += lds[L_RED + i * 128 + rh * 64 + lane];
    const f32x2 yf = *reinterpret_cast<const f32x2*>(&lds[L_YF + cb * 128 + 8 * c + 2 * r]);
    Acc8 ay; acc_zero(ay);
    static_for<0, 16>([&](auto t) {
      const float o1 = dpp<BCAST + t.v>(ov);
      const f32x2 o2 = {o1, o1};
#pragma unroll
      for (int k = 0; k < 4; ++k) ay.a[k] = __builtin_elementwise_fma(o2, wf[t.v].a[k], ay.a[k]);
    });
    float y0, y1;
    xrow_reduce8(ay, y0, y1);
    if (rh == 1) { y0 += yf[0]; y1 += yf[1]; }
    lds[L_QP + w * 64 + lane] = rh ? y0 : y1;
    wg_barrier();
    const float yo = lds[L_QP + (w ^ 4) * 64 + lane];
    const float ys = (rh ? y1 : y0) + yo;
    gu64* gp = (gu64*)(a.ws + WS_G) + ((size_t)(b * 64 + pidx)) * 512 + (cb * 2 + rh) * 64 + lane;
    __hip_atomic_store(gp, ((unsigned long long)tag << 32) | __float_as_uint(ys), RLX_AGENT);
  }
  if (w != 0) return;
  {
    const int P = pidx * 8 + (lane & 7), pl = P & 63;
    const int col = (P >> 7) * 128 + 8 * (pl & 15) + 2 * (pl >> 4) + ((P >> 6) & 1);
    const gu64* gp = (const gu64*)(a.ws + WS_G) + (size_t)(b * 64 + (lane >> 3)) * 512 + P;
    const float xv = lo16(__builtin_nontemporal_load(reinterpret_cast<const unsigned short*>(a.x) + b * DM + col));
    unsigned val[8];
    bool ok = false;
    __builtin_amdgcn_s_sleep(24);
    for (unsigned spins = 0; spins < SPIN_CAP; ++spins) {
      if (!ok) {
        bool o = true;
#pragma unroll
        for (int i = 0; i < 8; ++i) { const unsigned long long v = __hip_atomic_load(gp + i * 4096, RLX_AGENT); val[i] = (unsigned)v; o &= (unsigned)(v >> 32) == tag; }
        ok = o;
      }
      if (__all(ok)) break;
      __builtin_amdgcn_s_sleep(4);
    }
    float t = 0.f;
#pragma unroll
    for (int i = 0; i < 8; ++i) t += __uint_as_float(val[i]);
    t += dpp<ROR8>(t);
    t = swap16_add(t, t);
    t = swap32_add(t, t);
    if (lane < 8) reinterpret_cast<__bf16*>(a.out)[b * DM + col] = (__bf16)(xv + t);
    if (pidx == 0 && lane == 0) __hip_atomic_store((gu32*)(a.ws + WS_EPOCH + b * 256), tag, RLX_AGENT);
  }
}

extern "C" void kernel_launch(void* const* d_in, const int* in_sizes, int n_in, void* d_out, int out_size, void* d_ws, size_t ws_size,
                              hipStream_t stream) {
  Args a;
  a.x = (const char*)d_in[0]; a.k = (const char*)d_in[1]; a.v = (const char*)d_in[2]; a.wq = (const char*)d_in[3]; a.wo = (const char*)d_in[4];
  a.wg = (const char*)d_in[6]; a.wu = (const char*)d_in[7]; a.wd = (const char*)d_in[8];
  a.out = (char*)d_out; a.ws = (char*)d_ws;
  hipLaunchKernelGGL(fused_decode_kernel, dim3(NWG), dim3(NTHR), 0, stream, a);
}
```

```cpp
#include <hip/hip_runtime.h>
#include <hip/hip_bf16.h>
#include <stdint.h>

typedef unsigned u32x4 __attribute__((ext_vector_type(4)));
typedef unsigned u32x2 __attribute__((ext_vector_type(2)));
typedef float f32x2 __attribute__((ext_vector_type(2)));
typedef float f32x4 __attribute__((ext_vector_type(4)));
typedef __attribute__((ext_vector_type(2))) __bf16 bf16x2_t;
#define GAS __attribute__((address_space(1)))
typedef GAS unsigned gu32;
typedef GAS unsigned long long gu64;
#define RLX_AGENT __ATOMIC_RELAXED, __HIP_MEMORY_SCOPE_AGENT
#define RLX_WG __ATOMIC_RELAXED, __HIP_MEMORY_SCOPE_WORKGROUP

constexpr int DM = 512, NH = 32, SK = 8192, DH = 128, FF = 1792;
constexpr int NWG = 256, NTHR = 512;
constexpr int D = 8, DMID = 6, DLO = 4;
constexpr int R = 16;
constexpr int A = 50;
constexpr int HD = 8;
constexpr int NIT = 16;
constexpr int NUN = ((2 * (A - D) + 15) / 16) * 16;
constexpr int TAUK = 228, TAUV = 222, TMARG = 30;
constexpr unsigned SPIN_CAP = 1u << 18;
static_assert(D < R && NUN <= 16 * (NIT - 1), "schedule");
constexpr int joff(int J) { return (J >> 2) * 16384 + (J & 3) * 1024; }
__device__ constexpr size_t itoff(int it) { return (size_t)it << 17; }

constexpr size_t WS_EPOCH = 0;
constexpr size_t WS_LSE = 4096;
constexpr size_t WS_MEAN = 32768;
constexpr size_t WS_PROG = 65536;
constexpr size_t WS_G = 262144;

constexpr int L_QP = 0;
constexpr int L_RED = 512;
constexpr int L_GU = 1024;
constexpr int L_YF = 1280;
constexpr int L_CNT = 1792;
constexpr int L_VT = 1800;
constexpr int L_TAG = 1808;
constexpr int L_X = 2048;
constexpr int L_WO = 2304;
constexpr int L_FLOATS = L_WO + 32768;

template <int OFF> __device__ __forceinline__ void ld16(u32x4& d, unsigned voff, const void* sb) {
  asm volatile("global_load_dwordx4 %0, %1, %2 offset:%3 nt" : "=v"(d) : "v"(voff), "s"(sb), "i"(OFF) : "memory");
}
template <int N, int OFF> __device__ __forceinline__ void pace_ld(u32x4& nxt, unsigned voff, const void* sb) {
  asm volatile("s_waitcnt vmcnt(%3)\n\tglobal_load_dwordx4 %0, %1, %2 offset:%4 nt" : "=v"(nxt) : "v"(voff), "s"(sb), "i"(N), "i"(OFF) : "memory");
}
template <int N, int OFF> __device__ __forceinline__ void wait_ld(u32x4& cur, u32x4& nxt, unsigned voff, const void* sb) {
  asm volatile("s_waitcnt vmcnt(%4)\n\tglobal_load_dwordx4 %1, %2, %3 offset:%5 nt" : "+v"(cur), "+v"(nxt) : "v"(voff), "s"(sb), "i"(N), "i"(OFF) : "memory");
}
template <int OFF> __device__ __forceinline__ void wait_ld_lvl(u32x4& cur, u32x4& nxt, unsigned voff, const void* sb, int lvl) {
  asm volatile("s_cmp_lt_i32 %4, 1\n\ts_cbranch_scc1 .Lthr%=\n\ts_waitcnt vmcnt(%6)\n\ts_cmp_lt_i32 %4, 2\n\ts_cbranch_scc1 .Lthr%=\n\ts_waitcnt vmcnt(%7)\n.Lthr%=:\n\t"
               "s_waitcnt vmcnt(%5)\n\tglobal_load_dwordx4 %1, %2, %3 offset:%8 nt"
               : "+v"(cur), "+v"(nxt) : "v"(voff), "s"(sb), "s"(lvl), "i"(D - 1), "i"(DMID - 1), "i"(DLO - 1), "i"(OFF) : "memory", "scc");
}
#define SBAR() __builtin_amdgcn_sched_barrier(0)
template <int N> __device__ __forceinline__ void wait_vm(u32x4& r) { asm volatile("s_waitcnt vmcnt(%1)" : "+v"(r) : "i"(N) : "memory"); }
__device__ __forceinline__ void landed(u32x4& r) { asm volatile("" : "+v"(r)); }
__device__ __forceinline__ void wg_barrier() { asm volatile("s_waitcnt lgkmcnt(0)\n\ts_barrier" ::: "memory"); }

template <int CTRL> __device__ __forceinline__ float dpp(float x) {
  return __builtin_bit_cast(float, __builtin_amdgcn_mov_dpp(__builtin_bit_cast(int, x), CTRL, 0xf, 0xf, true));
}
constexpr int XOR1 = 0xB1, XOR2 = 0x4E, HMIR = 0x141, RMIR = 0x140, ROR4 = 0x124, ROR8 = 0x128, BCAST = 0x150;
__device__ __forceinline__ float lo16(unsigned u) { return __builtin_bit_cast(float, u << 16); }
__device__ __forceinline__ float hi16(unsigned u) { return __builtin_bit_cast(float, u & 0xffff0000u); }
__device__ __forceinline__ float dot2(unsigned k, unsigned q, float acc) {
  return __builtin_amdgcn_fdot2_f32_bf16(__builtin_bit_cast(bf16x2_t, k), __builtin_bit_cast(bf16x2_t, q), acc, false);
}
__device__ __forceinline__ unsigned pack_bf16(float lo, float hi) {
  bf16x2_t v = {(__bf16)lo, (__bf16)hi};
  return __builtin_bit_cast(unsigned, v);
}
__device__ __forceinline__ float swap32_add(float a, float b) {
  auto s = __builtin_amdgcn_permlane32_swap(__float_as_uint(a), __float_as_uint(b), false, false);
  return __uint_as_float(s[0]) + __uint_as_float(s[1]);
}
__device__ __forceinline__ float swap16_add(float a, float b) {
  auto s = __builtin_amdgcn_permlane16_swap(__float_as_uint(a), __float_as_uint(b), false, false);
  return __uint_as_float(s[0]) + __uint_as_float(s[1]);
}
__device__ __forceinline__ float swap32_max(float a) {
  auto s = __builtin_amdgcn_permlane32_swap(__float_as_uint(a), __float_as_uint(a), false, false);
  return fmaxf(__uint_as_float(s[0]), __uint_as_float(s[1]));
}
__device__ __forceinline__ float swap16_max(float a) {
  auto s = __builtin_amdgcn_permlane16_swap(__float_as_uint(a), __float_as_uint(a), false, false);
  return fmaxf(__uint_as_float(s[0]), __uint_as_float(s[1]));
}
__device__ __forceinline__ float row_sum(float x) { x += dpp<XOR1>(x); x += dpp<XOR2>(x); x += dpp<HMIR>(x); x += dpp<RMIR>(x); return x; }
__device__ __forceinline__ float row_max(float x) {
  x = fmaxf(x, dpp<XOR1>(x)); x = fmaxf(x, dpp<XOR2>(x)); x = fmaxf(x, dpp<HMIR>(x)); x = fmaxf(x, dpp<RMIR>(x)); return x;
}
__device__ __forceinline__ float wave_sum(float x) { x = row_sum(x); x = swap16_add(x, x); return swap32_add(x, x); }
__device__ __forceinline__ float wave_max(float x) { x = row_max(x); x = swap16_max(x); return swap32_max(x); }
struct Acc8 { f32x2 a[4]; };
__device__ __forceinline__ void acc_zero(Acc8& acc) {
#pragma unroll
  for (int k = 0; k < 4; ++k) acc.a[k] = f32x2{0.f, 0.f};
}
__device__ __forceinline__ void pin(Acc8& acc) { asm volatile("" : "+v"(acc.a[0]), "+v"(acc.a[1]), "+v"(acc.a[2]), "+v"(acc.a[3])); }
__device__ __forceinline__ void fma8(Acc8& acc, float s, const u32x4& w) {
  const f32x2 s2 = {s, s};
#pragma unroll
  for (int k = 0; k < 4; ++k) acc.a[k] = __builtin_elementwise_fma(s2, f32x2{lo16(w[k]), hi16(w[k])}, acc.a[k]);
  pin(acc);
}
__device__ __forceinline__ void unpack8(Acc8& o, const u32x4& w) {
#pragma unroll
  for (int k = 0; k < 4; ++k) o.a[k] = f32x2{lo16(w[k]), hi16(w[k])};
  pin(o);
}
__device__ __forceinline__ void xrow_reduce8(const Acc8& x, float& u0, float& u1) {
  const float t0 = swap32_add(x.a[0][0], x.a[2][0]), t1 = swap32_add(x.a[0][1], x.a[2][1]), t2 = swap32_add(x.a[1][0], x.a[3][0]), t3 = swap32_add(x.a[1][1], x.a[3][1]);
  u0 = swap16_add(t0, t2); u1 = swap16_add(t1, t3);
}
template <int T> struct IC { static constexpr int v = T; };
template <int I, int N, class F> __device__ __forceinline__ void static_for(F&& f) { if constexpr (I < N) { f(IC<I>{}); static_for<I + 1, N>(f); } }
__device__ __forceinline__ void lds_wait_count(float* lds, int idx, unsigned want) {
  for (unsigned spins = 0; spins < (SPIN_CAP << 2); ++spins) {
    const unsigned cnt = __hip_atomic_load(reinterpret_cast<unsigned*>(lds) + idx, RLX_WG);
    if (__builtin_amdgcn_readfirstlane(cnt) >= want) break;
    __builtin_amdgcn_s_sleep(1);
  }
  asm volatile("" ::: "memory");
}
__device__ __forceinline__ void lds_arrive(float* lds, int idx, int lane) {
  asm volatile("s_waitcnt lgkmcnt(0)" ::: "memory");
  if (lane == 0) __hip_atomic_fetch_add(reinterpret_cast<unsigned*>(lds) + idx, 1u, RLX_WG);
}
constexpr int cu_issued(int n) { return n < 2 * (A - D) ? A + n / 2 + 1 : n + D + 1; }

__device__ __forceinline__ void code_prefetch(int lane) {
  unsigned long long pc_; int diff_;
  asm volatile("s_getpc_b64 %0\n.Lpc%=:\n\ts_mov_b32 %1, .Lfused_decode_end-.Lpc%=" : "=s"(pc_), "=s"(diff_));
  const char* cp = (const char*)((pc_ + (long long)diff_ - 16384) & ~(unsigned long long)127);
  unsigned t0, t1;
  asm volatile("global_load_dword %0, %1, %2" : "=v"(t0) : "v"((unsigned)(lane * 128)), "s"(cp) : "memory");
  asm volatile("global_load_dword %0, %1, %2" : "=v"(t1) : "v"((unsigned)(lane * 128)), "s"(cp + 8192) : "memory");
  asm volatile("s_waitcnt vmcnt(0)" : "+v"(t0), "+v"(t1) :: "memory");
}

struct Args {
  const char* x; const char* k; const char* v; const char* wq; const char* wo; const char* wg; const char* wu; const char* wd;
  char* out; char* ws;
};

__global__ void __launch_bounds__(NTHR, 2) fused_decode_kernel(Args a) {
  __shared__ __attribute__((aligned(16))) float lds[L_FLOATS];
  const int tid = threadIdx.x, lane = tid & 63, w = __builtin_amdgcn_readfirstlane(tid >> 6);
  const int g = blockIdx.x, xcd = g & 7;
  const int s = (g >> 3) & 1, hh = (g >> 4) & 3, b = g >> 6, h = hh * 8 + xcd;
  const int r = lane >> 4, c = lane & 15;
  const int pidx = h * 2 + s;
  const int cb = w & 3, rh = w >> 2;
  Acc8 wf[16];
  unsigned tag;

  if (__builtin_expect(w < 4, 1)) {
    auto level = [&](int sched) -> int {
      const unsigned V = (unsigned)__builtin_amdgcn_s_memrealtime() - (unsigned)sched;
      unsigned* lv = reinterpret_cast<unsigned*>(lds) + L_VT;
      __hip_atomic_store(lv + w, V, RLX_WG);
      const unsigned vb_ = __hip_atomic_load(lv + 4, RLX_WG), ok = __hip_atomic_load(lv + 5, RLX_WG);
      const int d = (int)(V - vb_);
      return __builtin_amdgcn_readfirstlane(ok == 0u ? 1 : (d < -TMARG ? 2 : (d < TMARG ? 1 : 0)));
    };
    const size_t kvoff = (size_t)(b * NH + h) * SK * (DH * 2) + ((size_t)s << 16) + w * 4096;
    const char* kb = a.k + kvoff;
    const char* vb = a.v + kvoff;
    const unsigned vo = lane * 16;
    u32x4 win[NUN];
    u32x4 rs[R];
#pragma unroll
    for (int i = 0; i < R; ++i) rs[i] = u32x4{0u, 0u, 0u, 0u};
    static_for<0, A>([&](auto i) {
      constexpr int I = i.v;
      if constexpr (I < DMID) ld16<(joff(I & 15) & 4095)>(win[I], vo, kb + itoff(I >> 4) + (joff(I & 15) & ~4095));
      else pace_ld<DMID - 1, (joff(I & 15) & 4095)>(win[I], vo, kb + itoff(I >> 4) + (joff(I & 15) & ~4095));
    });
    wg_barrier();
    unsigned qh[4], ql[4];
    {
      float q0 = 0.f, q1 = 0.f;
#pragma unroll
      for (int i = 0; i < 4; ++i) { const f32x2 t = *reinterpret_cast<const f32x2*>(&lds[L_QP + i * 128 + 8 * c + 2 * r]); q0 += t[0]; q1 += t[1]; }
      constexpr float QS = 0.08838834764831845f * 1.4426950408889634f;
      q0 *= QS; q1 *= QS;
      const unsigned ph = pack_bf16(q0, q1);
      const unsigned pl = pack_bf16(q0 - lo16(ph), q1 - hi16(ph));
      {
        auto s1 = __builtin_amdgcn_permlane16_swap(ph, ph, false, false);
        auto e = __builtin_amdgcn_permlane32_swap(s1[0], s1[0], false, false);
        auto o = __builtin_amdgcn_permlane32_swap(s1[1], s1[1], false, false);
        qh[0] = e[0]; qh[2] = e[1]; qh[1] = o[0]; qh[3] = o[1];
      }
      {
        auto s1 = __builtin_amdgcn_permlane16_swap(pl, pl, false, false);
        auto e = __builtin_amdgcn_permlane32_swap(s1[0], s1[0], false, false);
        auto o = __builtin_amdgcn_permlane32_swap(s1[1], s1[1], false, false);
        ql[0] = e[0]; ql[2] = e[1]; ql[1] = o[0]; ql[3] = o[1];
      }
    }
    float sc[NIT];
#pragma unroll
    for (int i = 0; i < NIT; ++i) sc[i] = 0.f;
    const bool b0 = (c & 1) != 0, b1 = (c & 2) != 0;
    const int cq = c >> 2;
    auto reduce4 = [&](const float (&p)[4]) -> float {
      const float k0 = b0 ? p[1] : p[0], s0 = b0 ? p[0] : p[1];
      const float k1 = b0 ? p[3] : p[2], s1 = b0 ? p[2] : p[3];
      const float a0 = k0 + dpp<XOR1>(s0), a1 = k1 + dpp<XOR1>(s1);
      const float k2 = b1 ? a1 : a0, s2 = b1 ? a0 : a1;
      float t = k2 + dpp<XOR2>(s2);
      t += dpp<ROR4>(t); t += dpp<ROR8>(t);
      return t;
    };
    auto kdot = [&](const u32x4& kv) -> float {
      float d0 = dot2(kv[0], qh[0], 0.f), d1 = dot2(kv[0], ql[0], 0.f);
#pragma unroll
      for (int k = 1; k < 4; ++k) { d0 = dot2(kv[k], qh[k], d0); d1 = dot2(kv[k], ql[k], d1); }
      return d0 + d1;
    };
    {
      float part[4], snew = 0.f;
      static_for<0, NUN>([&](auto j) {
        constexpr int N = j.v;
        constexpr int I = cu_issued(N) - 1;
        constexpr bool issues = (N == 0) || (cu_issued(N) != cu_issued(N - 1));
        if constexpr (issues) {
          if constexpr (I < NUN) pace_ld<DMID - 1, (joff(I & 15) & 4095)>(win[I], vo, kb + itoff(I >> 4) + (joff(I & 15) & ~4095));
          else wait_ld<DMID - 1, (joff(I & 15) & 4095)>(win[N], rs[I % R], vo, kb + itoff(I >> 4) + (joff(I & 15) & ~4095));
        }
        landed(win[N]);
        part[N & 3] = kdot(win[N]);
        if constexpr ((N & 3) == 3) { const float t = reduce4(part); snew = (cq == ((N & 15) >> 2)) ? t : snew; }
        if constexpr ((N & 15) == 15) sc[NIT - NUN / 16 + N / 16] = snew;
        SBAR();
      });
    }
    for (int it = NUN / 16; it < NIT; ++it) {
      const char* pc = kb + itoff(it);
      const char* pn = it < NIT - 1 ? kb + itoff(it + 1) : vb;
      float part[4], snew = 0.f;
      const int lvl = level(it * TAUK);
      static_for<0, 16>([&](auto j) {
        constexpr int J = j.v;
        wait_ld_lvl<(joff((J + D) & 15) & 4095)>(rs[J % R], rs[(J + D) % R], vo, ((J + D >= 16) ? pn : pc) + (joff((J + D) & 15) & ~4095), lvl);
        part[J & 3] = kdot(rs[J % R]);
        if constexpr ((J & 3) == 3) { const float t = reduce4(part); snew = (cq == (J >> 2)) ? t : snew; }
        SBAR();
      });
#pragma unroll
      for (int i = 0; i < NIT - 1; ++i) sc[i] = sc[i + 1];
      sc[NIT - 1] = snew;
    }
    lds_wait_count(lds, L_CNT, 4u);
    tag = __builtin_amdgcn_readfirstlane(reinterpret_cast<const unsigned*>(lds)[L_TAG + b]);
    float mw;
    {
      float m = sc[0];
#pragma unroll
      for (int i = 1; i < NIT; ++i) m = fmaxf(m, sc[i]);
      mw = wave_max(m);
      float l = 0.f;
#pragma unroll
      for (int i = 0; i < NIT; ++i) { sc[i] = __builtin_amdgcn_exp2f(sc[i] - mw); l += sc[i]; }
      l = wave_sum(l);
      const float lse = mw + __builtin_amdgcn_logf(l);
      if (lane == 0)
        __hip_atomic_store((gu64*)(a.ws + WS_LSE) + (b * NH + h) * 8 + s * 4 + w, ((unsigned long long)tag << 32) | __float_as_uint(lse), RLX_AGENT);
    }
    Acc8 acc; acc_zero(acc);
    float coef = 0.f;
    for (int it = 0; it < NIT - 2; ++it) {
      const char* pc = vb + itoff(it);
      const char* pn = vb + itoff(it + 1);
      const float pcur = sc[0];
      int lvl = 1;
      static_for<0, 16>([&](auto j) {
        constexpr int J = j.v;
        if constexpr (J % 4 == 0) lvl = level(16 * TAUK + it * TAUV + J * TAUV / 16);
        wait_ld_lvl<(joff((J + D) & 15) & 4095)>(rs[J % R], rs[(J + D) % R], vo, ((J + D >= 16) ? pn : pc) + (joff((J + D) & 15) & ~4095), lvl);
        fma8(acc, dpp<BCAST + J>(pcur), rs[J % R]);
        SBAR();
      });
#pragma unroll
      for (int i = 0; i < NIT - 1; ++i) sc[i] = sc[i + 1];
    }
    {
      const char* pc = vb + itoff(NIT - 2);
      const char* pn = vb + itoff(NIT - 1);
      const float pcur = sc[0];
      lds_wait_count(lds, L_CNT + 1, 4u);
      if (lane == 0) __hip_atomic_fetch_add(reinterpret_cast<unsigned*>(lds) + L_VT + 6, 1u, RLX_WG);
      int lvl = 1;
      static_for<0, 16>([&](auto j) {
        constexpr int J = j.v;
        if constexpr (J % 4 == 0) lvl = level(16 * TAUK + (NIT - 2) * TAUV + J * TAUV / 16);
        const u32x4 wt = *reinterpret_cast<const u32x4*>(&lds[L_WO + (r * 16 + J) * 256 + cb * 64 + c * 4]);
        wait_ld_lvl<(joff((J + D) & 15) & 4095)>(rs[J % R], rs[(J + D) % R], vo, ((J + D >= 16) ? pn : pc) + (joff((J + D) & 15) & ~4095), lvl);
        fma8(acc, dpp<BCAST + J>(pcur), rs[J % R]);
        unpack8(wf[J], wt);
        SBAR();
      });
    }
    {
      u32x2 lg;
      const gu64* lsep = (const gu64*)(a.ws + WS_LSE) + (b * NH + h) * 8 + (c & 7);
      asm volatile("global_load_dwordx2 %0, %1, off sc1" : "=v"(lg) : "v"(lsep) : "memory");
      const char* pc = vb + itoff(NIT - 1);
      const float pcur = sc[1];
      const int lvl = level(16 * TAUK + (NIT - 1) * TAUV);
      static_for<0, 16>([&](auto j) {
        constexpr int J = j.v;
        if constexpr (J + D < 16) wait_ld_lvl<(joff((J + D) & 15) & 4095)>(rs[J % R], rs[(J + D) % R], vo, pc + (joff((J + D) & 15) & ~4095), lvl);
        else wait_vm<15 - J>(rs[J % R]);
        if constexpr (J == D + 1) {
          asm volatile("" : "+v"(lg));
          unsigned lv = lg[0], lt = lg[1];
          for (unsigned spins = 0; spins < SPIN_CAP; ++spins) {
            if (__all(lt == tag)) break;
            __builtin_amdgcn_s_sleep(8);
            const unsigned long long v = __hip_atomic_load(lsep, RLX_AGENT);
            lv = (unsigned)v; lt = (unsigned)(v >> 32);
          }
          const float le = __uint_as_float(lv);
          const float M = row_max(le);
          const float L = 0.5f * row_sum(__builtin_amdgcn_exp2f(le - M));
          coef = __builtin_amdgcn_exp2f(mw - M) / L;
        }
        fma8(acc, dpp<BCAST + J>(pcur), rs[J % R]);
        SBAR();
      });
    }
    float u0, u1;
    xrow_reduce8(acc, u0, u1);
    *reinterpret_cast<f32x2*>(&lds[L_RED + w * 128 + 8 * c + 2 * r]) = f32x2{u0 * coef, u1 * coef};
  } else {
    const int hw = w - 4;
    const int cg = hh * 2 + s;
    const int f0 = xcd * 224 + (cg < 7 ? cg : 6) * 32;
    u32x4 xc; unsigned xo0, xo1;
    {
      const char* xb = a.x + b * (DM * 2);
      asm volatile("global_load_dwordx4 %0, %1, %2" : "=v"(xc) : "v"((unsigned)(lane * 16)), "s"(xb) : "memory");
      asm volatile("global_load_ushort %0, %1, %2" : "=v"(xo0) : "v"((unsigned)(lane * 2)), "s"(xb + hw * 256) : "memory");
      asm volatile("global_load_ushort %0, %1, %2 offset:128" : "=v"(xo1) : "v"((unsigned)(lane * 2)), "s"(xb + hw * 256) : "memory");
    }
    const char* wqb = a.wq + (size_t)(hw * 128) * (NH * DH * 2) + h * (DH * 2);
    const unsigned voq = (unsigned)(r * 16) * (NH * DH * 2) + c * 16;
    u32x4 wt[32];
    static_for<0, HD>([&](auto t) { ld16<0>(wt[t.v], voq, wqb + ((t.v >> 4) * 64 + (t.v & 15)) * (NH * DH * 2)); });
    if (tid == 256) { lds[L_CNT] = 0.f; lds[L_CNT + 1] = 0.f; lds[L_VT + 5] = 0.f; lds[L_VT + 6] = 0.f; }
    asm volatile("s_waitcnt vmcnt(%3)" : "+v"(xc), "+v"(xo0), "+v"(xo1) : "i"(HD) : "memory");
    float xn[2];
    {
      if (hw == 0) *reinterpret_cast<u32x4*>(&lds[L_X + lane * 4]) = xc;
      float xv[8];
#pragma unroll
      for (int k = 0; k < 4; ++k) { xv[2 * k] = lo16(xc[k]); xv[2 * k + 1] = hi16(xc[k]); }
      float sm = ((xv[0] + xv[1]) + (xv[2] + xv[3])) + ((xv[4] + xv[5]) + (xv[6] + xv[7]));
      const float mean = wave_sum(sm) * (1.0f / DM);
      float sq = 0.f;
#pragma unroll
      for (int k = 0; k < 8; ++k) { const float d = xv[k] - mean; sq = fmaf(d, d, sq); }
      const float var = wave_sum(sq) * (1.0f / DM);
      const float rstd = 1.0f / sqrtf(var + 1e-5f);
      xn[0] = (lo16(xo0) - mean) * rstd;
      xn[1] = (lo16(xo1) - mean) * rstd;
    }
    {
      Acc8 acc; acc_zero(acc);
      SBAR();
      static_for<0, 32>([&](auto t) {
        constexpr int T = t.v, I = T + HD;
        wait_vm<(I <= 32 ? HD - 1 : 31 - T)>(wt[T]);
        if constexpr (I < 32) ld16<0>(wt[I], voq, wqb + ((I >> 4) * 64 + (I & 15)) * (NH * DH * 2));
        fma8(acc, dpp<BCAST + (T & 15)>(xn[T >> 4]), wt[T]);
        SBAR();
      });
      float u0, u1;
      xrow_reduce8(acc, u0, u1);
      *reinterpret_cast<f32x2*>(&lds[L_QP + hw * 128 + 8 * c + 2 * r]) = f32x2{u0, u1};
    }
    wg_barrier();
    if (hw == 0 && lane < 4) reinterpret_cast<unsigned*>(lds)[L_TAG + lane] = __hip_atomic_load((const gu32*)(a.ws + WS_EPOCH + lane * 256), RLX_AGENT) + 1u;
    {
      Acc8 ag, au; acc_zero(ag); acc_zero(au);
      const unsigned vog = (unsigned)((lane >> 2) * 4) * (FF * 2) + (lane & 3) * 16;
      const size_t sog = (size_t)(hw * 128) * (FF * 2) + f0 * 2;
      static_for<0, 8>([&](auto t) {
        constexpr int T = t.v;
        u32x4 gt, ut;
        ld16<0>(gt, vog, a.wg + sog + ((T >> 2) * 64 + (T & 3)) * (FF * 2));
        ld16<0>(ut, vog, a.wu + sog + ((T >> 2) * 64 + (T & 3)) * (FF * 2));
        const float xb_ = dpp<(T & 3) * 0x55>(xn[T >> 2]);
        wait_vm<1>(gt); fma8(ag, xb_, gt);
        wait_vm<0>(ut); fma8(au, xb_, ut);
        SBAR();
      });
      float t8[8];
#pragma unroll
      for (int k = 0; k < 8; ++k) t8[k] = swap32_add(ag.a[k >> 1][k & 1], au.a[k >> 1][k & 1]);
#pragma unroll
      for (int k = 0; k < 4; ++k) {
        float t = swap16_add(t8[k], t8[k + 4]); t += dpp<ROR4>(t); t += dpp<ROR8>(t);
        if ((lane & 12) == 0) lds[L_GU + ((r >> 1) * 32 + 8 * (lane & 3) + 4 * (r & 1) + k) * 4 + hw] = t;
      }
      lds_arrive(lds, L_CNT, lane);
      u32x4 wd[8];
      const char* wdb = a.wd + (size_t)f0 * (DM * 2) + hw * 256;
      const unsigned vod = (unsigned)(r * 8) * (DM * 2) + c * 16;
      static_for<0, 8>([&](auto t) {
        constexpr int T = t.v;
        if constexpr (T < 2) ld16<(T & 3) * 1024>(wd[T], vod, wdb + (T >> 2) * 4096);
        else pace_ld<1, (T & 3) * 1024>(wd[T], vod, wdb + (T >> 2) * 4096);
      });
      lds_wait_count(lds, L_CNT, 4u);
      const int f = r * 8 + (c & 7);
      const f32x4 g4 = *reinterpret_cast<const f32x4*>(&lds[L_GU + f * 4]), u4 = *reinterpret_cast<const f32x4*>(&lds[L_GU + (32 + f) * 4]);
      const float gs = (g4[0] + g4[1]) + (g4[2] + g4[3]), us = (u4[0] + u4[1]) + (u4[2] + u4[3]);
      const float hv = gs / (1.0f + __expf(-gs)) * us;
      Acc8 ay; acc_zero(ay);
      asm volatile("s_waitcnt vmcnt(0)" : "+v"(wd[0]), "+v"(wd[1]), "+v"(wd[2]), "+v"(wd[3]), "+v"(wd[4]), "+v"(wd[5]), "+v"(wd[6]), "+v"(wd[7]) :: "memory");
      if (cg < 7) static_for<0, 8>([&](auto t) { fma8(ay, dpp<BCAST + t.v>(hv), wd[t.v]); });
      float y0, y1;
      xrow_reduce8(ay, y0, y1);
      *reinterpret_cast<f32x2*>(&lds[L_YF + hw * 128 + 8 * c + 2 * r]) = f32x2{y0, y1};
    }
    {
      const char* wo0 = a.wo + (size_t)(h * DH + hw * 32) * (DM * 2);
      for (int i = 0; i < 32; i += 2) {
        u32x4 t0, t1;
        ld16<0>(t0, lane * 16, wo0 + i * 1024);
        ld16<1024>(t1, lane * 16, wo0 + i * 1024);
        wait_vm<1>(t0);
        *reinterpret_cast<u32x4*>(&lds[L_WO + (hw * 32 + i) * 256 + lane * 4]) = t0;
        wait_vm<0>(t1);
        *reinterpret_cast<u32x4*>(&lds[L_WO + (hw * 32 + i + 1) * 256 + lane * 4]) = t1;
      }
      lds_arrive(lds, L_CNT + 1, lane);
      lds_wait_count(lds, L_CNT + 1, 4u);
      static_for<0, 16>([&](auto t) { unpack8(wf[t.v], *reinterpret_cast<const u32x4*>(&lds[L_WO + (64 + r * 16 + t.v) * 256 + cb * 64 + c * 4])); });
    }
    if (hw == 2) code_prefetch(lane);
    tag = __builtin_amdgcn_readfirstlane(reinterpret_cast<const unsigned*>(lds)[L_TAG + b]);
    if (hw == 0) {
      unsigned* lv = reinterpret_cast<unsigned*>(lds) + L_VT;
      unsigned tg4[4];
#pragma unroll
      for (int i = 0; i < 4; ++i) tg4[i] = reinterpret_cast<const unsigned*>(lds)[L_TAG + i];
      gu64* pg = (gu64*)(a.ws + WS_PROG);
      gu64* pm = (gu64*)(a.ws + WS_MEAN);
      for (unsigned spins = 0; spins < (1u << 16); ++spins) {
        if (__builtin_amdgcn_readfirstlane(__hip_atomic_load(lv + 6, RLX_WG)) != 0u) break;
        const unsigned now = (unsigned)__builtin_amdgcn_s_memrealtime() >> 8;
        const unsigned v0 = __hip_atomic_load(lv, RLX_WG), v1 = __hip_atomic_load(lv + 1, RLX_WG), v2 = __hip_atomic_load(lv + 2, RLX_WG), v3 = __hip_atomic_load(lv + 3, RLX_WG);
        const unsigned v = v0 + (unsigned)(((int)(v1 - v0) + (int)(v2 - v0) + (int)(v3 - v0)) >> 2);
        if (lane == 0) __hip_atomic_store(pg + (now & 63u) * 256 + g, ((unsigned long long)tag << 32) | v, RLX_AGENT);
        if (g == 0) {
          float sd = 0.f, sn = 0.f;
          const gu64* pr = pg + ((now - 1u) & 63u) * 256 + lane;
#pragma unroll
          for (int i = 0; i < 4; ++i) {
            const unsigned long long x = __hip_atomic_load(pr + i * 64, RLX_AGENT);
            const bool ok = (unsigned)(x >> 32) == tg4[i];
            sd += ok ? (float)(int)((unsigned)x - v) : 0.f; sn += ok ? 1.f : 0.f;
          }
          sd = wave_sum(sd); sn = wave_sum(sn);
          if (lane < 32 && sn >= 128.f) __hip_atomic_store(pm + lane * 64, ((unsigned long long)tag << 32) | (v + (unsigned)(int)(sd / sn)), RLX_AGENT);
        }
        const unsigned long long m = __hip_atomic_load(pm + (g & 31) * 64, RLX_AGENT);
        if (lane == 0 && (unsigned)(m >> 32) == tg4[0]) { __hip_atomic_store(lv + 4, (unsigned)m, RLX_WG); __hip_atomic_store(lv + 5, 1u, RLX_WG); }
        __builtin_amdgcn_s_sleep(48);
      }
    }
  }
  wg_barrier();
  {
    float ov = 0.f;
#pragma unroll
    for (int i = 0; i < 4; ++i) ov += lds[L_RED + i * 128 + rh * 64 + lane];
    const f32x2 yf = *reinterpret_cast<const f32x2*>(&lds[L_YF + cb * 128 + 8 * c + 2 * r]);
    Acc8 ay; acc_zero(ay);
    static_for<0, 16>([&](auto t) {
      const float o1 = dpp<BCAST + t.v>(ov);
      const f32x2 o2 = {o1, o1};
#pragma unroll
      for (int k = 0; k < 4; ++k) ay.a[k] = __builtin_elementwise_fma(o2, wf[t.v].a[k], ay.a[k]);
    });
    float y0, y1;
    xrow_reduce8(ay, y0, y1);
    if (rh == 1) { y0 += yf[0]; y1 += yf[1]; }
    lds[L_QP + w * 64 + lane] = rh ? y0 : y1;
    wg_barrier();
    const float yo = lds[L_QP + (w ^ 4) * 64 + lane];
    const float ys = (rh ? y1 : y0) + yo;
    gu64* gp = (gu64*)(a.ws + WS_G) + ((size_t)(b * 64 + pidx)) * 512 + (cb * 2 + rh) * 64 + lane;
    __hip_atomic_store(gp, ((unsigned long long)tag << 32) | __float_as_uint(ys), RLX_AGENT);
  }
  if (w != 0) return;
  {
    const int P = pidx * 8 + (lane & 7), pl = P & 63;
    const int col = (P >> 7) * 128 + 8 * (pl & 15) + 2 * (pl >> 4) + ((P >> 6) & 1);
    const gu64* gp = (const gu64*)(a.ws + WS_G) + (size_t)(b * 64 + (lane >> 3)) * 512 + P;
    const float xv = lo16(reinterpret_cast<const unsigned short*>(&lds[L_X])[col]);
    unsigned val[8];
    bool ok = false;
    __builtin_amdgcn_s_sleep(16);
    for (unsigned spins = 0; spins < SPIN_CAP; ++spins) {
      if (!ok) {
        bool o = true;
#pragma unroll
        for (int i = 0; i < 8; ++i) { const unsigned long long v = __hip_atomic_load(gp + i * 4096, RLX_AGENT); val[i] = (unsigned)v; o &= (unsigned)(v >> 32) == tag; }
        ok = o;
      }
      if (__all(ok)) break;
      __builtin_amdgcn_s_sleep(1);
    }
    float t = 0.f;
#pragma unroll
    for (int i = 0; i < 8; ++i) t += __uint_as_float(val[i]);
    t += dpp<ROR8>(t);
    t = swap16_add(t, t);
    t = swap32_add(t, t);
    if (lane < 8) reinterpret_cast<__bf16*>(a.out)[b * DM + col] = (__bf16)(xv + t);
    if (pidx == 0 && lane == 0) __hip_atomic_store((gu32*)(a.ws + WS_EPOCH + b * 256), tag, RLX_AGENT);
    asm volatile(".Lfused_decode_end:");
  }
}

extern "C" void kernel_launch(void* const* d_in, const int* in_sizes, int n_in, void* d_out, int out_size, void* d_ws, size_t ws_size,
                              hipStream_t stream) {
  Args a;
  a.x = (const char*)d_in[0]; a.k = (const char*)d_in[1]; a.v = (const char*)d_in[2]; a.wq = (const char*)d_in[3]; a.wo = (const char*)d_in[4];
  a.wg = (const char*)d_in[6]; a.wu = (const char*)d_in[7]; a.wd = (const char*)d_in[8];
  a.out = (char*)d_out; a.ws = (char*)d_ws;
  hipLaunchKernelGGL(fused_decode_kernel, dim3(NWG), dim3(NTHR), 0, stream, a);
}
```
